```python
import torch

def _softmax(x, axis=-1):
    return torch.softmax(x, dim=axis)
def _gelu(x):
    return torch.nn.functional.gelu(x, approximate="tanh")
def _silu(x):
    return torch.nn.functional.silu(x)
def _sigmoid(x):
    return torch.sigmoid(x)
def _layernorm(x, axis=-1, eps=1e-5):
    m = x.mean(dim=axis, keepdim=True)
    v = x.var(dim=axis, keepdim=True, unbiased=False)
    return (x - m) / torch.sqrt(v + eps)
def _scatter_add(tgt, idx, src, axis=0):
    return tgt.clone().index_add_(axis, idx, src)
def _segment_sum(src, seg, n):
    out = torch.zeros((n,) + src.shape[1:], dtype=src.dtype)
    return out.index_add_(0, seg, src)
def _pad(x, pw):
    flat = tuple(v for pair in reversed(pw) for v in pair)
    return torch.nn.functional.pad(x, flat)

kernel_name = 'standalone__paged_block_table_kv_radixattention__sampling'

def setup_inputs():

    _w = (lambda a: (lambda t: t.to(torch.bfloat16) if t.is_floating_point() else t)(torch.as_tensor(a)))
    B, H, Hkv, Dh = 32, 16, 8, 256
    Lk = 2048
    bs = 16
    nb = max(2, Lk // bs)
    P = max(1, nb // 2)
    n_phys = P + B * (nb - P)
    shared = torch.arange(P, dtype=torch.int64)
    priv = P + torch.arange(B * (nb - P), dtype=torch.int64).reshape(B, nb - P)
    bt = torch.cat([torch.tile(shared[None, :], (B, 1)), priv], dim=1)
    ctx = nb * bs - (torch.arange(1, B + 1, dtype=torch.int64) * 2654435761) % max(1, (nb * bs - P * bs) // 2)
    return {
        "q": _w((lambda *s: torch.randn(*s, dtype=torch.bfloat16) * 0.8)(B, H, Dh)),
        "k_cache": _w((lambda *s: torch.randn(*s, dtype=torch.bfloat16) * 0.8)(n_phys, bs, Hkv, Dh)),
        "v_cache": _w((lambda *s: torch.randn(*s, dtype=torch.bfloat16) * 0.05)(n_phys, bs, Hkv, Dh)),
        "block_table": _w(bt.to(torch.int64)),
        "ctx_lens": _w(torch.as_tensor(ctx).to(torch.int64)),
    }

def reference(q, k_cache, v_cache, block_table, ctx_lens):
    B, H, Dh = q.shape
    n_phys, bs, Hkv, _ = k_cache.shape
    nb = block_table.shape[1]
    Lk = nb * bs
    rep = H // Hkv
    k = k_cache[block_table].reshape(B, Lk, Hkv, Dh)
    v = v_cache[block_table].reshape(B, Lk, Hkv, Dh)
    k = (lambda x, n, axis: torch.repeat_interleave(x, n, dim=axis))(k, rep, axis=2)
    v = (lambda x, n, axis: torch.repeat_interleave(x, n, dim=axis))(v, rep, axis=2)
    scores = torch.einsum("bhd,bkhd->bhk", q, k) * (Dh ** -0.5)
    pos = torch.arange(Lk)
    valid = pos[None, :] < ctx_lens[:, None]
    scores = torch.where(valid[:, None, :], scores, -1e9)
    p = _softmax(scores, axis=-1)
    return torch.einsum("bhk,bkhd->bhd", p, v)
```

```cpp
#include <hip/hip_runtime.h>
#include <hip/hip_bf16.h>
#include <cstdint>

constexpr int B = 32, H = 16, HKV = 8, D = 256, BS = 16, NB = 128, LK = NB * BS;
constexpr int PAGE_BYTES = BS * HKV * D * 2;
constexpr int ROW_BYTES = HKV * D * 2;
constexpr int NW = 8, NWG = 256;
constexpr int NSH = 4, NPR = 3, NSLOT = 8;
constexpr int NPC = 4;
#define CS_NUM 3
#define CS_DEN 2
#define CS_ADD 5
constexpr float LOG2E = 1.4426950408889634f;
constexpr float C2 = 0.0625f * LOG2E;
constexpr float MASKED = -998244352.0f;
constexpr size_t WS_ML = 0, WS_ACC = (size_t)B * H * NSLOT * 2 * 4;

using bf16x8 = __attribute__((ext_vector_type(8))) __bf16;
using u32x4 = __attribute__((ext_vector_type(4))) unsigned;
using f32x4 = __attribute__((ext_vector_type(4))) float;
using f32x2 = __attribute__((ext_vector_type(2))) float;
using bf16x2 = __attribute__((ext_vector_type(2))) __bf16;

template <int NT> __device__ __forceinline__ u32x4 ld16(const char* p) {
  if (NT) return __builtin_nontemporal_load(reinterpret_cast<const u32x4*>(p));
  return *reinterpret_cast<const u32x4*>(p);
}
__device__ __forceinline__ unsigned cvtpk(float lo, float hi) {
  return __builtin_bit_cast(unsigned, __builtin_convertvector(f32x2{lo, hi}, bf16x2));
}
__device__ __forceinline__ float xrow16_max(float x) {
  auto s = __builtin_amdgcn_permlane16_swap(__float_as_uint(x), __float_as_uint(x), false, false);
  x = fmaxf(__uint_as_float(s[0]), __uint_as_float(s[1]));
  auto t = __builtin_amdgcn_permlane32_swap(__float_as_uint(x), __float_as_uint(x), false, false);
  return fmaxf(__uint_as_float(t[0]), __uint_as_float(t[1]));
}
__device__ __forceinline__ float xrow16_sum(float x) {
  auto s = __builtin_amdgcn_permlane16_swap(__float_as_uint(x), __float_as_uint(x), false, false);
  x = __uint_as_float(s[0]) + __uint_as_float(s[1]);
  auto t = __builtin_amdgcn_permlane32_swap(__float_as_uint(x), __float_as_uint(x), false, false);
  return __uint_as_float(t[0]) + __uint_as_float(t[1]);
}

struct Tile { u32x4 k[8]; u32x4 v[8]; };

template <int NT> __device__ __forceinline__ void issue(Tile& t, const char* kp, const char* vp) {
#pragma unroll
  for (int j = 0; j < 8; ++j) t.k[j] = ld16<NT>(kp + j * 2 * ROW_BYTES);
#pragma unroll
  for (int j = 0; j < 8; ++j) t.v[j] = ld16<NT>(vp + j * 2 * ROW_BYTES);
}

struct LaneC { int wr_off, rd_off, tok0; unsigned pmask; };

__device__ __forceinline__ void kwrite(const u32x4 (&k)[8], char* ks, const LaneC& lc) {
#pragma unroll
  for (int j = 0; j < 8; ++j) *reinterpret_cast<u32x4*>(ks + j * 1024 + (lc.wr_off ^ (j * 16))) = k[j];
}
__device__ __forceinline__ void core(const u32x4 (&v)[8], const u32x4 (&q)[8], int rem, const char* ks, const LaneC& lc,
                                     float& m, float& l, f32x4 (&acc)[8]) {
  f32x4 s0 = {0.f, 0.f, 0.f, 0.f}, s1 = {0.f, 0.f, 0.f, 0.f};
#pragma unroll
  for (int j = 0; j < 4; ++j) {
    const u32x4 ka = *reinterpret_cast<const u32x4*>(ks + (lc.rd_off ^ (2 * j * 64)));
    const u32x4 kb = *reinterpret_cast<const u32x4*>(ks + (lc.rd_off ^ ((2 * j + 1) * 64)));
    s0 = __builtin_amdgcn_mfma_f32_16x16x32_bf16(__builtin_bit_cast(bf16x8, ka), __builtin_bit_cast(bf16x8, q[2 * j]), s0, 0, 0, 0);
    s1 = __builtin_amdgcn_mfma_f32_16x16x32_bf16(__builtin_bit_cast(bf16x8, kb), __builtin_bit_cast(bf16x8, q[2 * j + 1]), s1, 0, 0, 0);
  }
  float x[4];
#pragma unroll
  for (int i = 0; i < 4; ++i) {
    x[i] = (s0[i] + s1[i]) * C2;
    if (rem < BS) x[i] = (lc.tok0 + 2 * i < rem) ? x[i] : -INFINITY;
  }
  float mx = fmaxf(fmaxf(x[0], x[1]), fmaxf(x[2], x[3]));
  mx = xrow16_max(mx);
  const float mn = fmaxf(m, mx);
  const float msub = (mn == -INFINITY) ? 0.f : mn;
  const float alpha = __builtin_amdgcn_exp2f(m - msub);
  float p[4];
#pragma unroll
  for (int i = 0; i < 4; ++i) p[i] = __builtin_amdgcn_exp2f(x[i] - msub);
  l = fmaf(l, alpha, (p[0] + p[1]) + (p[2] + p[3]));
  if (__builtin_amdgcn_ballot_w64(mn != m) != 0) {
#pragma unroll
    for (int u = 0; u < 8; ++u) acc[u] *= alpha;
  }
  m = mn;
  const unsigned p01 = cvtpk(p[0], p[1]), p23 = cvtpk(p[2], p[3]);
  auto sa = __builtin_amdgcn_permlane16_swap(p01, p01, false, false);
  auto sb = __builtin_amdgcn_permlane16_swap(p23, p23, false, false);
  const u32x4 pb = {sa[0] & lc.pmask, sb[0] & lc.pmask, sa[1] & lc.pmask, sb[1] & lc.pmask};
#pragma unroll
  for (int d = 0; d < 8; ++d) {
    const unsigned sel = (d & 1) ? 0x07060302u : 0x05040100u;
    const u32x4 a = {__builtin_amdgcn_perm(v[1][d >> 1], v[0][d >> 1], sel), __builtin_amdgcn_perm(v[3][d >> 1], v[2][d >> 1], sel),
                     __builtin_amdgcn_perm(v[5][d >> 1], v[4][d >> 1], sel), __builtin_amdgcn_perm(v[7][d >> 1], v[6][d >> 1], sel)};
    acc[d] = __builtin_amdgcn_mfma_f32_16x16x32_bf16(__builtin_bit_cast(bf16x8, a), __builtin_bit_cast(bf16x8, pb), acc[d], 0, 0, 0);
  }
}

__global__ void __launch_bounds__(NW * 64, 2)
attn_split(const char* __restrict__ qg, const char* __restrict__ kc, const char* __restrict__ vc,
           const int* __restrict__ bt, const int* __restrict__ lens, char* __restrict__ ws) {
  __shared__ __attribute__((aligned(16))) char big[131072];
  __shared__ __attribute__((aligned(16))) int lds_bt[B * NB];
  __shared__ __attribute__((aligned(16))) char lds_q[NPC][1728];
  __shared__ int lds_min[NW];
  __shared__ float lds_ml[NPC][NW][2][2];
  const int wg = blockIdx.x, tid = threadIdx.x, lane = tid & 63, wave = __builtin_amdgcn_readfirstlane(tid >> 6);
  const int g = lane >> 4, c = lane & 15;
  float* ws_ml = reinterpret_cast<float*>(ws + WS_ML);
  float* ws_acc = reinterpret_cast<float*>(ws + WS_ACC);
  int len = lens[lane & 31];
  {
    const int col = (tid & 15) * 8;
    const u32x4 a0 = *reinterpret_cast<const u32x4*>(bt + tid * 8), a1 = *reinterpret_cast<const u32x4*>(bt + tid * 8 + 4);
    const u32x4 r0 = *reinterpret_cast<const u32x4*>(bt + col), r1 = *reinterpret_cast<const u32x4*>(bt + col + 4);
    *reinterpret_cast<u32x4*>(lds_bt + tid * 8) = a0; *reinterpret_cast<u32x4*>(lds_bt + tid * 8 + 4) = a1;
    int fm = NB;
#pragma unroll
    for (int e = 3; e >= 0; --e) if (a1[e] != r1[e]) fm = col + 4 + e;
#pragma unroll
    for (int e = 3; e >= 0; --e) if (a0[e] != r0[e]) fm = col + e;
#pragma unroll
    for (int o = 32; o >= 1; o >>= 1) fm = min(fm, __shfl_xor(fm, o));
    if (lane == 0) lds_min[wave] = fm;
    if (wave < NPC && lane < 32) *reinterpret_cast<u32x4*>(lds_q[wave] + 2 * 576 + lane * 16) = u32x4{0u, 0u, 0u, 0u};
  }
  len = min(max(len, 0), LK);
  int minlen = len;
#pragma unroll
  for (int o = 16; o >= 1; o >>= 1) minlen = min(minlen, __shfl_xor(minlen, o));
  __syncthreads();
  int S = minlen / BS;
#pragma unroll
  for (int w = 0; w < NW; ++w) S = min(S, lds_min[w]);
  S = __builtin_amdgcn_readfirstlane(S);
  const int priv = (lane < 32) ? (len + BS - 1) / BS - S : 0;
  int incl = priv, mxp = priv;
#pragma unroll
  for (int o = 1; o < 32; o <<= 1) { const int t = __shfl_up(incl, o); if (lane >= o) incl += t; }
#pragma unroll
  for (int o = 16; o >= 1; o >>= 1) mxp = max(mxp, __shfl_xor(mxp, o));
  const int Tp = 8 * __builtin_amdgcn_readlane(incl, 31);
  mxp = __builtin_amdgcn_readfirstlane(mxp);
  const int Ns = S > 0 ? 8 * NSH : 0, per = (S + NSH - 1) / NSH, cs = S > 0 ? per * CS_NUM / CS_DEN + CS_ADD : 0;
  const int umin = max((mxp + NPR - 2) / (NPR - 1), 1);
  int U = max((Tp + Ns * cs + NWG - 1) / NWG, umin), Us = U - cs;
  if (Us < umin) { Us = 0; U = max((Tp + (NWG - Ns) - 1) / (NWG - Ns), umin); }
  const int usplit = Ns * Us;
  auto wg_of = [&](int u) { return u < usplit ? u / Us : Ns + (u - usplit) / U; };
  {
    const int b = wg >> 3, kvh = wg & 7;
    const int pb = __builtin_amdgcn_readlane(priv, b), st = 8 * (__builtin_amdgcn_readlane(incl, b) - pb) + kvh * pb;
    const int npc = pb > 0 ? wg_of(st + pb - 1) - wg_of(st) + 1 : 0;
    const int slot = wave;
    if ((slot < NPR && slot >= npc) || (slot >= NPR && slot < NPR + NSH && Ns == 0)) {
#pragma unroll
      for (int h = 0; h < 2; ++h) {
        const size_t rs = (size_t)(b * H + 2 * kvh + h) * NSLOT + slot;
        *reinterpret_cast<f32x4*>(ws_acc + rs * D + lane * 4) = f32x4{0.f, 0.f, 0.f, 0.f};
        if (lane == 0) *reinterpret_cast<f32x2*>(ws_ml + rs * 2) = f32x2{-INFINITY, 0.f};
      }
    }
  }
  LaneC lc;
  lc.wr_off = (lane >> 5) * 512 + (((lane & 31) ^ (8 * (lane >> 5))) * 16);
  {
    const int tk = 8 * ((c >> 2) & 1) + 2 * (c & 3) + (c >> 3);
    lc.rd_off = tk * 512 + ((g ^ c) * 16);
  }
  lc.tok0 = 8 * (g & 1) + (g >> 1);
  lc.pmask = ((g & 1) == (c & 1)) ? 0xffffffffu : 0u;
  const int loff = (lane >> 5) * ROW_BYTES + (lane & 31) * 16;
  const char* kl = kc + loff;
  const char* vl = vc + loff;
  u32x4 q[8];
  float m, l;
  f32x4 acc[8];
  Tile ta, tb;
  if (wg < Ns) {
    const int kvh = wg & 7, sp = wg >> 3;
    const int p0 = min(sp * per, S), n = min(p0 + per, S) - p0;
    const int qr = c >> 1, row = (4 * wave + (qr >> 1)) * H + 2 * kvh + (qr & 1);
    if (n > 0) { const size_t pb_ = (size_t)lds_bt[p0 + min(wave, n - 1)] * PAGE_BYTES + kvh * 512; issue<1>(ta, kl + pb_, vl + pb_); }
#pragma unroll
    for (int j = 0; j < 8; ++j) q[j] = *reinterpret_cast<const u32x4*>(qg + (size_t)row * (D * 2) + (4 * j + g) * 16);
    m = -INFINITY; l = 0.f;
#pragma unroll
    for (int u = 0; u < 8; ++u) acc[u] = f32x4{0.f, 0.f, 0.f, 0.f};
    for (int r = 0; r < n; r += NW) {
      char* st = big + wave * 16384;
      kwrite(ta.k, st, lc);
#pragma unroll
      for (int j = 0; j < 8; ++j) *reinterpret_cast<u32x4*>(st + 8192 + j * 1024 + lane * 16) = ta.v[j];
      { const size_t pb_ = (size_t)lds_bt[p0 + min(r + NW + wave, n - 1)] * PAGE_BYTES + kvh * 512; if (r + NW < n) issue<1>(ta, kl + pb_, vl + pb_); }
      __syncthreads();
      const int cnt = min(NW, n - r);
      for (int pg = 0; pg < cnt; ++pg) {
        const char* sp_ = big + pg * 16384;
#pragma unroll
        for (int j = 0; j < 8; ++j) tb.v[j] = *reinterpret_cast<const u32x4*>(sp_ + 8192 + j * 1024 + lane * 16);
        core(tb.v, q, BS, sp_, lc, m, l, acc);
      }
      __syncthreads();
    }
    l = xrow16_sum(l);
    const size_t rs = (size_t)row * NSLOT + NPR + sp;
#pragma unroll
    for (int i = 0; i < 4; ++i) {
      float* dst = ws_acc + rs * D + 128 * (c & 1) + 32 * g + 8 * i;
      *reinterpret_cast<f32x4*>(dst) = f32x4{acc[0][i], acc[1][i], acc[2][i], acc[3][i]};
      *reinterpret_cast<f32x4*>(dst + 4) = f32x4{acc[4][i], acc[5][i], acc[6][i], acc[7][i]};
    }
    if (g == 0 && (c & 1) == 0) *reinterpret_cast<f32x2*>(ws_ml + rs * 2) = f32x2{m, l};
  }
  char* ks = big + wave * 8192;
  const int qoff = min(c >> 1, 2) * 576 + g * 16;
  const char* qs = lds_q[0];
  int u = wg < Ns ? wg * Us : usplit + (wg - Ns) * U;
  const int u1 = min(u + (wg < Ns ? Us : U), Tp);
  while (u < u1) {
    const int uu = u + wave + NW * lane;
    int offv = 0, remv = BS, pcv = NPC, ue = u, rowp = 0, slotp = 0, npcs = 0;
#pragma unroll
    for (int p = 0; p < NPC; ++p) {
      if (ue < u1) {
        const int b = __builtin_ctzll(__builtin_amdgcn_ballot_w64(lane < 32 && 8 * incl > ue));
        const int pb = __builtin_amdgcn_readlane(priv, b), st = 8 * (__builtin_amdgcn_readlane(incl, b) - pb);
        const int lb = __builtin_amdgcn_readlane(len, b);
        const int off = ue - st, kvh = off / pb, pg0 = off - kvh * pb, np = min(pb - pg0, u1 - ue);
        if (uu >= ue && uu < ue + np) {
          const int lp = S + pg0 + (uu - ue);
          offv = lds_bt[b * NB + lp] * PAGE_BYTES + kvh * 512; remv = lb - lp * BS; pcv = p;
        }
        if (wave == p) *reinterpret_cast<u32x4*>(lds_q[p] + (lane >> 5) * 576 + (lane & 31) * 16) = *reinterpret_cast<const u32x4*>(qg + (size_t)(b * H + 2 * kvh) * (D * 2) + lane * 16);
        if ((tid >> 7) == p) { rowp = b * H + 2 * kvh; slotp = wg - wg_of(st + kvh * pb); }
        ue += np; npcs = p + 1;
      }
    }
    const int n = (ue - u > wave) ? (ue - u - wave + NW - 1) / NW : 0;
    if (lane < NPC * 2) { lds_ml[lane >> 1][wave][lane & 1][0] = -INFINITY; lds_ml[lane >> 1][wave][lane & 1][1] = 0.f; }
#define ISSUE(T, I) do { const unsigned pb_ = (unsigned)__builtin_amdgcn_readlane(offv, (I)); issue<1>(T, kl + pb_, vl + pb_); } while (0)
#define LOADQ(P) do { qs = lds_q[P] + qoff; m = -INFINITY; l = 0.f; _Pragma("unroll") for (int v = 0; v < 8; ++v) acc[v] = f32x4{0.f, 0.f, 0.f, 0.f}; } while (0)
#define FLUSH(P) do { l = xrow16_sum(l); if (c < 4) { \
      float* mrg = reinterpret_cast<float*>(big + 65536 + ((P) * NW + wave) * 2048) + (c >> 1) * D + 128 * (c & 1) + 32 * g; \
      _Pragma("unroll") for (int i = 0; i < 4; ++i) { \
        *reinterpret_cast<f32x4*>(mrg + 8 * i) = f32x4{acc[0][i], acc[1][i], acc[2][i], acc[3][i]}; \
        *reinterpret_cast<f32x4*>(mrg + 8 * i + 4) = f32x4{acc[4][i], acc[5][i], acc[6][i], acc[7][i]}; } \
      if (g == 0 && (c & 1) == 0) { lds_ml[P][wave][c >> 1][0] = m; lds_ml[P][wave][c >> 1][1] = l; } } } while (0)
#define COMPUTE(T, I) do { kwrite(T.k, ks, lc); _Pragma("unroll") for (int j = 0; j < 8; ++j) q[j] = *reinterpret_cast<const u32x4*>(qs + j * 64); core(T.v, q, __builtin_amdgcn_readlane(remv, (I)), ks, lc, m, l, acc); \
      const int pn_ = __builtin_amdgcn_readlane(pcv, (I) + 1); if (pn_ != pc) { FLUSH(pc); pc = pn_; if (pc < NPC) LOADQ(pc); } } while (0)
    if (n > 0) ISSUE(ta, 0);
    __syncthreads();
    if (n > 0) {
      int pc = __builtin_amdgcn_readlane(pcv, 0);
      LOADQ(pc);
      int i = 0;
      for (; i + 2 < n; i += 2) {
        ISSUE(tb, i + 1);
        COMPUTE(ta, i);
        ISSUE(ta, i + 2);
        COMPUTE(tb, i + 1);
      }
      if (n - i == 2) {
        ISSUE(tb, i + 1);
        COMPUTE(ta, i);
        COMPUTE(tb, i + 1);
      } else {
        COMPUTE(ta, i);
      }
    }
#undef ISSUE
#undef LOADQ
#undef FLUSH
#undef COMPUTE
    __syncthreads();
    {
      const int p = tid >> 7, h = (tid >> 6) & 1;
      if (p < npcs) {
        float M = -INFINITY;
#pragma unroll
        for (int w = 0; w < NW; ++w) M = fmaxf(M, lds_ml[p][w][h][0]);
        float L = 0.f; f32x4 o = {0.f, 0.f, 0.f, 0.f};
#pragma unroll
        for (int w = 0; w < NW; ++w) {
          const float lw = lds_ml[p][w][h][1];
          if (lw > 0.f) {
            const float wt = __builtin_amdgcn_exp2f(lds_ml[p][w][h][0] - M);
            L = fmaf(wt, lw, L); o += *reinterpret_cast<const f32x4*>(big + 65536 + (p * NW + w) * 2048 + h * 1024 + lane * 16) * wt;
          }
        }
        const size_t rs = (size_t)(rowp + h) * NSLOT + slotp;
        *reinterpret_cast<f32x4*>(ws_acc + rs * D + lane * 4) = o;
        if (lane == 0) *reinterpret_cast<f32x2*>(ws_ml + rs * 2) = f32x2{M, L};
      }
    }
    u = ue;
    if (u < u1) __syncthreads();
  }
}

__global__ void __launch_bounds__(256)
attn_combine(const char* __restrict__ ws, const char* __restrict__ vc, const int* __restrict__ bt, const int* __restrict__ lens,
             __hip_bfloat16* __restrict__ out) {
  const int lane = threadIdx.x & 63, row = blockIdx.x * 4 + (threadIdx.x >> 6);
  const float* ml = reinterpret_cast<const float*>(ws + WS_ML) + (size_t)row * NSLOT * 2;
  const float* ac = reinterpret_cast<const float*>(ws + WS_ACC) + (size_t)row * NSLOT * D + lane * 4;
  f32x2 mlv[7]; f32x4 a[7];
#pragma unroll
  for (int s = 0; s < 7; ++s) { mlv[s] = *reinterpret_cast<const f32x2*>(ml + 2 * s); a[s] = *reinterpret_cast<const f32x4*>(ac + s * D); }
  const int b = row / H;
  const int len = min(max(lens[b], 0), LK);
  float M = -INFINITY;
#pragma unroll
  for (int s = 0; s < 7; ++s) M = fmaxf(M, mlv[s][0]);
  float L = 0.f; f32x4 o = {0.f, 0.f, 0.f, 0.f};
#pragma unroll
  for (int s = 0; s < 7; ++s) {
    const float wt = (mlv[s][1] > 0.f) ? __builtin_amdgcn_exp2f(mlv[s][0] - M) : 0.f;
    L = fmaf(wt, mlv[s][1], L); o += a[s] * wt;
  }
  const float MK = MASKED * LOG2E;
  if (len < LK && !(M > MK + 200.f)) {
    const float M2 = fmaxf(M, MK);
    const float w1 = (L > 0.f) ? __builtin_amdgcn_exp2f(M - M2) : 0.f, w2 = __builtin_amdgcn_exp2f(MK - M2);
    f32x4 sv = {0.f, 0.f, 0.f, 0.f};
    const int kvh = (row % H) >> 1;
    for (int t = len; t < LK; ++t) {
      const ushort4 r = *reinterpret_cast<const ushort4*>(vc + (size_t)bt[b * NB + (t >> 4)] * PAGE_BYTES + (size_t)(t & 15) * ROW_BYTES + kvh * 512 + lane * 8);
      sv[0] += __uint_as_float((unsigned)r.x << 16); sv[1] += __uint_as_float((unsigned)r.y << 16);
      sv[2] += __uint_as_float((unsigned)r.z << 16); sv[3] += __uint_as_float((unsigned)r.w << 16);
    }
    o = o * w1 + sv * w2; L = L * w1 + (float)(LK - len) * w2;
  }
  const float inv = 1.f / L;
  uint2 r = {cvtpk(o[0] * inv, o[1] * inv), cvtpk(o[2] * inv, o[3] * inv)};
  *reinterpret_cast<uint2*>(out + (size_t)row * D + lane * 4) = r;
}

extern "C" void kernel_launch(void* const* d_in, const int* in_sizes, int n_in, void* d_out, int out_size,
                              void* d_ws, size_t ws_size, hipStream_t stream) {
  attn_split<<<NWG, NW * 64, 0, stream>>>((const char*)d_in[0], (const char*)d_in[1], (const char*)d_in[2],
                                          (const int*)d_in[3], (const int*)d_in[4], (char*)d_ws);
  attn_combine<<<B * H / 4, 256, 0, stream>>>((const char*)d_ws, (const char*)d_in[2], (const int*)d_in[3], (const int*)d_in[4],
                                              (__hip_bfloat16*)d_out);
}
```

```cpp
#include <hip/hip_runtime.h>
#include <hip/hip_bf16.h>
#include <cstdint>

constexpr int B = 32, H = 16, HKV = 8, D = 256, BS = 16, NB = 128, LK = NB * BS;
constexpr int PAGE_BYTES = BS * HKV * D * 2;
constexpr int ROW_BYTES = HKV * D * 2;
constexpr int NW = 8, NWG = 256;
constexpr int NSH = 4, NPR = 3, NSLOT = 8;
constexpr int NPC = 4;
#define CS_NUM 3
#define CS_DEN 2
#define CS_ADD 5
constexpr float LOG2E = 1.4426950408889634f;
constexpr float C2 = 0.0625f * LOG2E;
constexpr float MASKED = -998244352.0f;
constexpr size_t WS_ML = 0, WS_ACC = (size_t)B * H * NSLOT * 2 * 4;

using bf16x8 = __attribute__((ext_vector_type(8))) __bf16;
using u32x4 = __attribute__((ext_vector_type(4))) unsigned;
using f32x4 = __attribute__((ext_vector_type(4))) float;
using f32x2 = __attribute__((ext_vector_type(2))) float;
using bf16x2 = __attribute__((ext_vector_type(2))) __bf16;

template <int NT> __device__ __forceinline__ u32x4 ld16(const char* p) {
  if (NT) return __builtin_nontemporal_load(reinterpret_cast<const u32x4*>(p));
  return *reinterpret_cast<const u32x4*>(p);
}
__device__ __forceinline__ unsigned cvtpk(float lo, float hi) {
  return __builtin_bit_cast(unsigned, __builtin_convertvector(f32x2{lo, hi}, bf16x2));
}
__device__ __forceinline__ float xrow16_max(float x) {
  auto s = __builtin_amdgcn_permlane16_swap(__float_as_uint(x), __float_as_uint(x), false, false);
  x = fmaxf(__uint_as_float(s[0]), __uint_as_float(s[1]));
  auto t = __builtin_amdgcn_permlane32_swap(__float_as_uint(x), __float_as_uint(x), false, false);
  return fmaxf(__uint_as_float(t[0]), __uint_as_float(t[1]));
}
__device__ __forceinline__ float xrow16_sum(float x) {
  auto s = __builtin_amdgcn_permlane16_swap(__float_as_uint(x), __float_as_uint(x), false, false);
  x = __uint_as_float(s[0]) + __uint_as_float(s[1]);
  auto t = __builtin_amdgcn_permlane32_swap(__float_as_uint(x), __float_as_uint(x), false, false);
  return __uint_as_float(t[0]) + __uint_as_float(t[1]);
}

struct Tile { u32x4 k[8]; u32x4 v[8]; };

template <int CTRL> __device__ __forceinline__ int dpp_i(int x, int fill) { return __builtin_amdgcn_update_dpp(fill, x, CTRL, 0xf, 0xf, false); }
template <int CTRL, int RM> __device__ __forceinline__ int dpp_m(int x, int fill) { return __builtin_amdgcn_update_dpp(fill, x, CTRL, RM, 0xf, false); }
__device__ __forceinline__ int scan_add(int x) {
  x += dpp_i<0x111>(x, 0); x += dpp_i<0x112>(x, 0); x += dpp_i<0x114>(x, 0); x += dpp_i<0x118>(x, 0);
  x += dpp_m<0x142, 0xa>(x, 0); x += dpp_m<0x143, 0xc>(x, 0); return x;
}
__device__ __forceinline__ int scan_min(int x) {
  constexpr int F = 0x7fffffff;
  x = min(x, dpp_i<0x111>(x, F)); x = min(x, dpp_i<0x112>(x, F)); x = min(x, dpp_i<0x114>(x, F)); x = min(x, dpp_i<0x118>(x, F));
  x = min(x, dpp_m<0x142, 0xa>(x, F)); x = min(x, dpp_m<0x143, 0xc>(x, F)); return x;
}

template <int NT> __device__ __forceinline__ void issue(Tile& t, const char* kp, const char* vp) {
#pragma unroll
  for (int j = 0; j < 8; ++j) t.k[j] = ld16<NT>(kp + j * 2 * ROW_BYTES);
#pragma unroll
  for (int j = 0; j < 8; ++j) t.v[j] = ld16<NT>(vp + j * 2 * ROW_BYTES);
}

struct LaneC { int wr_off, rd_off, tok0; unsigned pmask; };

__device__ __forceinline__ void kwrite(const u32x4 (&k)[8], char* ks, const LaneC& lc) {
#pragma unroll
  for (int j = 0; j < 8; ++j) *reinterpret_cast<u32x4*>(ks + j * 1024 + (lc.wr_off ^ (j * 16))) = k[j];
}
__device__ __forceinline__ void core(const u32x4 (&v)[8], const u32x4 (&q)[8], int rem, const char* ks, const LaneC& lc,
                                     float& m, float& l, f32x4 (&acc)[8]) {
  f32x4 s0 = {0.f, 0.f, 0.f, 0.f}, s1 = {0.f, 0.f, 0.f, 0.f};
#pragma unroll
  for (int j = 0; j < 4; ++j) {
    const u32x4 ka = *reinterpret_cast<const u32x4*>(ks + (lc.rd_off ^ (2 * j * 64)));
    const u32x4 kb = *reinterpret_cast<const u32x4*>(ks + (lc.rd_off ^ ((2 * j + 1) * 64)));
    s0 = __builtin_amdgcn_mfma_f32_16x16x32_bf16(__builtin_bit_cast(bf16x8, ka), __builtin_bit_cast(bf16x8, q[2 * j]), s0, 0, 0, 0);
    s1 = __builtin_amdgcn_mfma_f32_16x16x32_bf16(__builtin_bit_cast(bf16x8, kb), __builtin_bit_cast(bf16x8, q[2 * j + 1]), s1, 0, 0, 0);
  }
  float x[4];
#pragma unroll
  for (int i = 0; i < 4; ++i) {
    x[i] = (s0[i] + s1[i]) * C2;
    if (rem < BS) x[i] = (lc.tok0 + 2 * i < rem) ? x[i] : -INFINITY;
  }
  float mx = fmaxf(fmaxf(x[0], x[1]), fmaxf(x[2], x[3]));
  mx = xrow16_max(mx);
  const float mn = fmaxf(m, mx);
  const float msub = (mn == -INFINITY) ? 0.f : mn;
  const float alpha = __builtin_amdgcn_exp2f(m - msub);
  float p[4];
#pragma unroll
  for (int i = 0; i < 4; ++i) p[i] = __builtin_amdgcn_exp2f(x[i] - msub);
  l = fmaf(l, alpha, (p[0] + p[1]) + (p[2] + p[3]));
  if (__builtin_amdgcn_ballot_w64(mn != m) != 0) {
#pragma unroll
    for (int u = 0; u < 8; ++u) acc[u] *= alpha;
  }
  m = mn;
  const unsigned p01 = cvtpk(p[0], p[1]), p23 = cvtpk(p[2], p[3]);
  auto sa = __builtin_amdgcn_permlane16_swap(p01, p01, false, false);
  auto sb = __builtin_amdgcn_permlane16_swap(p23, p23, false, false);
  const u32x4 pb = {sa[0] & lc.pmask, sb[0] & lc.pmask, sa[1] & lc.pmask, sb[1] & lc.pmask};
#pragma unroll
  for (int d = 0; d < 8; ++d) {
    const unsigned sel = (d & 1) ? 0x07060302u : 0x05040100u;
    const u32x4 a = {__builtin_amdgcn_perm(v[1][d >> 1], v[0][d >> 1], sel), __builtin_amdgcn_perm(v[3][d >> 1], v[2][d >> 1], sel),
                     __builtin_amdgcn_perm(v[5][d >> 1], v[4][d >> 1], sel), __builtin_amdgcn_perm(v[7][d >> 1], v[6][d >> 1], sel)};
    acc[d] = __builtin_amdgcn_mfma_f32_16x16x32_bf16(__builtin_bit_cast(bf16x8, a), __builtin_bit_cast(bf16x8, pb), acc[d], 0, 0, 0);
  }
}

__global__ void __launch_bounds__(NW * 64, 2)
attn_split(const char* __restrict__ qg, const char* __restrict__ kc, const char* __restrict__ vc,
           const int* __restrict__ bt, const int* __restrict__ lens, char* __restrict__ ws) {
  __shared__ __attribute__((aligned(16))) char big[131072];
  __shared__ __attribute__((aligned(16))) int lds_bt[B * NB];
  __shared__ __attribute__((aligned(16))) char lds_q[NPC][1728];
  __shared__ __attribute__((aligned(16))) int lds_min[NW];
  __shared__ float lds_ml[NPC][NW][2][2];
  const int wg = blockIdx.x, tid = threadIdx.x, lane = tid & 63, wave = __builtin_amdgcn_readfirstlane(tid >> 6);
  const int g = lane >> 4, c = lane & 15;
#define STAMP(I)
  STAMP(0);
  float* ws_ml = reinterpret_cast<float*>(ws + WS_ML);
  float* ws_acc = reinterpret_cast<float*>(ws + WS_ACC);
  int len = lens[lane & 31];
  {
    const int col = (tid & 15) * 8;
    const u32x4 a0 = *reinterpret_cast<const u32x4*>(bt + tid * 8), a1 = *reinterpret_cast<const u32x4*>(bt + tid * 8 + 4);
    const u32x4 r0 = *reinterpret_cast<const u32x4*>(bt + col), r1 = *reinterpret_cast<const u32x4*>(bt + col + 4);
    *reinterpret_cast<u32x4*>(lds_bt + tid * 8) = a0; *reinterpret_cast<u32x4*>(lds_bt + tid * 8 + 4) = a1;
    int fm = NB;
#pragma unroll
    for (int e = 3; e >= 0; --e) if (a1[e] != r1[e]) fm = col + 4 + e;
#pragma unroll
    for (int e = 3; e >= 0; --e) if (a0[e] != r0[e]) fm = col + e;
    fm = scan_min(fm);
    if (lane == 63) lds_min[wave] = fm;
    if (wave < NPC && lane < 32) *reinterpret_cast<u32x4*>(lds_q[wave] + 2 * 576 + lane * 16) = u32x4{0u, 0u, 0u, 0u};
  }
  len = min(max(len, 0), LK);
  const int npg = (len + BS - 1) / BS;
  const int minlen = __builtin_amdgcn_readlane(scan_min(len), 31), mxn = -__builtin_amdgcn_readlane(scan_min(-npg), 31);
  const int incn = scan_add(npg);
  __syncthreads();
  int S = minlen / BS;
  {
    const int4 m0 = *reinterpret_cast<const int4*>(lds_min), m1 = *reinterpret_cast<const int4*>(lds_min + 4);
    S = min(min(min(S, m0.x), min(m0.y, m0.z)), min(min(m0.w, m1.x), min(min(m1.y, m1.z), m1.w)));
  }
  S = __builtin_amdgcn_readfirstlane(S);
  STAMP(1);
  const int priv = npg - S;
  const int incl = incn - S * (lane + 1);
  const int Tp = 8 * __builtin_amdgcn_readlane(incl, 31), mxp = mxn - S;
  const int Ns = S > 0 ? 8 * NSH : 0, per = (S + NSH - 1) / NSH, cs = S > 0 ? per * CS_NUM / CS_DEN + CS_ADD : 0;
  const int umin = max((mxp + NPR - 2) / (NPR - 1), 1);
  int U = max((Tp + Ns * cs + NWG - 1) / NWG, umin), Us = U - cs;
  if (Us < umin) { Us = 0; U = max((Tp + (NWG - Ns) - 1) / (NWG - Ns), umin); }
  const int usplit = Ns * Us;
  auto wg_of = [&](int u) { return u < usplit ? u / Us : Ns + (u - usplit) / U; };
  LaneC lc;
  lc.wr_off = (lane >> 5) * 512 + (((lane & 31) ^ (8 * (lane >> 5))) * 16);
  {
    const int tk = 8 * ((c >> 2) & 1) + 2 * (c & 3) + (c >> 3);
    lc.rd_off = tk * 512 + ((g ^ c) * 16);
  }
  lc.tok0 = 8 * (g & 1) + (g >> 1);
  lc.pmask = ((g & 1) == (c & 1)) ? 0xffffffffu : 0u;
  const int loff = (lane >> 5) * ROW_BYTES + (lane & 31) * 16;
  const char* kl = kc + loff;
  const char* vl = vc + loff;
  u32x4 q[8];
  float m, l;
  f32x4 acc[8];
  Tile ta, tb;
  if (wg < Ns) {
    const int kvh = wg & 7, sp = wg >> 3;
    const int p0 = min(sp * per, S), n = min(p0 + per, S) - p0;
    const int qr = c >> 1, row = (4 * wave + (qr >> 1)) * H + 2 * kvh + (qr & 1);
    if (n > 0) { const size_t pb_ = (size_t)lds_bt[p0 + min(wave, n - 1)] * PAGE_BYTES + kvh * 512; issue<1>(ta, kl + pb_, vl + pb_); }
#pragma unroll
    for (int j = 0; j < 8; ++j) q[j] = *reinterpret_cast<const u32x4*>(qg + (size_t)row * (D * 2) + (4 * j + g) * 16);
    m = -INFINITY; l = 0.f;
#pragma unroll
    for (int u = 0; u < 8; ++u) acc[u] = f32x4{0.f, 0.f, 0.f, 0.f};
    for (int r = 0; r < n; r += NW) {
      char* st = big + wave * 16384;
      kwrite(ta.k, st, lc);
#pragma unroll
      for (int j = 0; j < 8; ++j) *reinterpret_cast<u32x4*>(st + 8192 + j * 1024 + lane * 16) = ta.v[j];
      { const size_t pb_ = (size_t)lds_bt[p0 + min(r + NW + wave, n - 1)] * PAGE_BYTES + kvh * 512; if (r + NW < n) issue<1>(ta, kl + pb_, vl + pb_); }
      __syncthreads();
      const int cnt = min(NW, n - r);
      for (int pg = 0; pg < cnt; ++pg) {
        const char* sp_ = big + pg * 16384;
#pragma unroll
        for (int j = 0; j < 8; ++j) tb.v[j] = *reinterpret_cast<const u32x4*>(sp_ + 8192 + j * 1024 + lane * 16);
        core(tb.v, q, BS, sp_, lc, m, l, acc);
      }
      __syncthreads();
    }
    l = xrow16_sum(l);
    const size_t rs = (size_t)row * NSLOT + NPR + sp;
#pragma unroll
    for (int i = 0; i < 4; ++i) {
      float* dst = ws_acc + rs * D + 128 * (c & 1) + 32 * g + 8 * i;
      *reinterpret_cast<f32x4*>(dst) = f32x4{acc[0][i], acc[1][i], acc[2][i], acc[3][i]};
      *reinterpret_cast<f32x4*>(dst + 4) = f32x4{acc[4][i], acc[5][i], acc[6][i], acc[7][i]};
    }
    if (g == 0 && (c & 1) == 0) *reinterpret_cast<f32x2*>(ws_ml + rs * 2) = f32x2{m, l};
  }
  STAMP(2);
  char* ks = big + wave * 8192;
  const int qoff = min(c >> 1, 2) * 576 + g * 16;
  const char* qs = lds_q[0];
  int u = wg < Ns ? wg * Us : usplit + (wg - Ns) * U;
  const int u1 = min(u + (wg < Ns ? Us : U), Tp);
  bool first = true;
  while (u < u1) {
    const int uu = u + wave + NW * lane;
    int offv = 0, remv = BS, pcv = NPC, ue = u, rowp = 0, slotp = 0, npcs = 0, qrow = 0;
#pragma unroll
    for (int p = 0; p < NPC; ++p) {
      if (ue < u1) {
        const int b = __builtin_ctzll(__builtin_amdgcn_ballot_w64(lane < 32 && 8 * incl > ue));
        const int pb = __builtin_amdgcn_readlane(priv, b), st = 8 * (__builtin_amdgcn_readlane(incl, b) - pb);
        const int lb = __builtin_amdgcn_readlane(len, b);
        const int off = ue - st, kvh = off / pb, pg0 = off - kvh * pb, np = min(pb - pg0, u1 - ue);
        if (uu >= ue && uu < ue + np) {
          const int lp = S + pg0 + (uu - ue);
          offv = lds_bt[b * NB + lp] * PAGE_BYTES + kvh * 512; remv = lb - lp * BS; pcv = p;
        }
        if (wave == p) qrow = b * H + 2 * kvh;
        if ((tid >> 7) == p) { rowp = b * H + 2 * kvh; slotp = wg - wg_of(st + kvh * pb); }
        ue += np; npcs = p + 1;
      }
    }
    const u32x4 qst = *reinterpret_cast<const u32x4*>(qg + (size_t)qrow * (D * 2) + lane * 16);
    const int n = (ue - u > wave) ? (ue - u - wave + NW - 1) / NW : 0;
    if (lane < NPC * 2) { lds_ml[lane >> 1][wave][lane & 1][0] = -INFINITY; lds_ml[lane >> 1][wave][lane & 1][1] = 0.f; }
#define ISSUE(T, I) do { const unsigned pb_ = (unsigned)__builtin_amdgcn_readlane(offv, (I)); issue<1>(T, kl + pb_, vl + pb_); } while (0)
#define LOADQ(P) do { qs = lds_q[P] + qoff; m = -INFINITY; l = 0.f; _Pragma("unroll") for (int v = 0; v < 8; ++v) acc[v] = f32x4{0.f, 0.f, 0.f, 0.f}; } while (0)
#define FLUSH(P) do { l = xrow16_sum(l); if (c < 4) { \
      float* mrg = reinterpret_cast<float*>(big + 65536 + ((P) * NW + wave) * 2048) + (c >> 1) * D + 128 * (c & 1) + 32 * g; \
      _Pragma("unroll") for (int i = 0; i < 4; ++i) { \
        *reinterpret_cast<f32x4*>(mrg + 8 * i) = f32x4{acc[0][i], acc[1][i], acc[2][i], acc[3][i]}; \
        *reinterpret_cast<f32x4*>(mrg + 8 * i + 4) = f32x4{acc[4][i], acc[5][i], acc[6][i], acc[7][i]}; } \
      if (g == 0 && (c & 1) == 0) { lds_ml[P][wave][c >> 1][0] = m; lds_ml[P][wave][c >> 1][1] = l; } } } while (0)
#define COMPUTE
    if (n > 0) ISSUE(ta, 0);
    if (wave < npcs) *reinterpret_cast<u32x4*>(lds_q[wave] + (lane >> 5) * 576 + (lane & 31) * 16) = qst;
    if (first) { first = false;
      {
        const int b = wg >> 3, kvh = wg & 7;
        const int pb = __builtin_amdgcn_readlane(priv, b), st = 8 * (__builtin_amdgcn_readlane(incl, b) - pb) + kvh * pb;
        const int npc = pb > 0 ? wg_of(st + pb - 1) - wg_of(st) + 1 : 0;
        const int slot = wave;
        if ((slot < NPR && slot >= npc) || (slot >= NPR && slot < NPR + NSH && Ns == 0)) {
    #pragma unroll
          for (int h = 0; h < 2; ++h) {
            const size_t rs = (size_t)(b * H + 2 * kvh + h) * NSLOT + slot;
            *reinterpret_cast<f32x4*>(ws_acc + rs * D + lane * 4) = f32x4{0.f, 0.f, 0.f, 0.f};
            if (lane == 0) *reinterpret_cast<f32x2*>(ws_ml + rs * 2) = f32x2{-INFINITY, 0.f};
          }
        }
      }
     }
    __syncthreads();
    STAMP(3);
    if (n > 0) {
      int pc = __builtin_amdgcn_readlane(pcv, 0);
      LOADQ(pc);
      for (int i = 0; i < n; ++i) {
        kwrite(ta.k, ks, lc);
#pragma unroll
        for (int j = 0; j < 8; ++j) tb.v[j] = ta.v[j];
        if (i + 1 < n) ISSUE(ta, i + 1);
#pragma unroll
        for (int j = 0; j < 8; ++j) q[j] = *reinterpret_cast<const u32x4*>(qs + j * 64);
        core(tb.v, q, __builtin_amdgcn_readlane(remv, i), ks, lc, m, l, acc);
        const int pn = __builtin_amdgcn_readlane(pcv, i + 1);
        if (pn != pc) { FLUSH(pc); pc = pn; if (pc < NPC) LOADQ(pc); }
      }
    }
#undef ISSUE
#undef LOADQ
#undef FLUSH
#undef COMPUTE
    STAMP(4);
    __syncthreads();
    STAMP(5);
    {
      const int p = tid >> 7, h = (tid >> 6) & 1;
      if (p < npcs) {
        float M = -INFINITY;
#pragma unroll
        for (int w = 0; w < NW; ++w) M = fmaxf(M, lds_ml[p][w][h][0]);
        float L = 0.f; f32x4 o = {0.f, 0.f, 0.f, 0.f};
#pragma unroll
        for (int w = 0; w < NW; ++w) {
          const float lw = lds_ml[p][w][h][1];
          if (lw > 0.f) {
            const float wt = __builtin_amdgcn_exp2f(lds_ml[p][w][h][0] - M);
            L = fmaf(wt, lw, L); o += *reinterpret_cast<const f32x4*>(big + 65536 + (p * NW + w) * 2048 + h * 1024 + lane * 16) * wt;
          }
        }
        const size_t rs = (size_t)(rowp + h) * NSLOT + slotp;
        *reinterpret_cast<f32x4*>(ws_acc + rs * D + lane * 4) = o;
        if (lane == 0) *reinterpret_cast<f32x2*>(ws_ml + rs * 2) = f32x2{M, L};
      }
    }
    u = ue;
    if (u < u1) __syncthreads();
  }
  if (first) {
  {
    const int b = wg >> 3, kvh = wg & 7;
    const int pb = __builtin_amdgcn_readlane(priv, b), st = 8 * (__builtin_amdgcn_readlane(incl, b) - pb) + kvh * pb;
    const int npc = pb > 0 ? wg_of(st + pb - 1) - wg_of(st) + 1 : 0;
    const int slot = wave;
    if ((slot < NPR && slot >= npc) || (slot >= NPR && slot < NPR + NSH && Ns == 0)) {
#pragma unroll
      for (int h = 0; h < 2; ++h) {
        const size_t rs = (size_t)(b * H + 2 * kvh + h) * NSLOT + slot;
        *reinterpret_cast<f32x4*>(ws_acc + rs * D + lane * 4) = f32x4{0.f, 0.f, 0.f, 0.f};
        if (lane == 0) *reinterpret_cast<f32x2*>(ws_ml + rs * 2) = f32x2{-INFINITY, 0.f};
      }
    }
  }
  }
  STAMP(6);
}

__global__ void __launch_bounds__(256)
attn_combine(const char* __restrict__ ws, const char* __restrict__ vc, const int* __restrict__ bt, const int* __restrict__ lens,
             __hip_bfloat16* __restrict__ out) {
  const int lane = threadIdx.x & 63, row = blockIdx.x * 4 + (threadIdx.x >> 6);
  const float* ml = reinterpret_cast<const float*>(ws + WS_ML) + (size_t)row * NSLOT * 2;
  const float* ac = reinterpret_cast<const float*>(ws + WS_ACC) + (size_t)row * NSLOT * D + lane * 4;
  f32x2 mlv[7]; f32x4 a[7];
#pragma unroll
  for (int s = 0; s < 7; ++s) { mlv[s] = *reinterpret_cast<const f32x2*>(ml + 2 * s); a[s] = *reinterpret_cast<const f32x4*>(ac + s * D); }
  const int b = row / H;
  const int len = min(max(lens[b], 0), LK);
  float M = -INFINITY;
#pragma unroll
  for (int s = 0; s < 7; ++s) M = fmaxf(M, mlv[s][0]);
  float L = 0.f; f32x4 o = {0.f, 0.f, 0.f, 0.f};
#pragma unroll
  for (int s = 0; s < 7; ++s) {
    const float wt = (mlv[s][1] > 0.f) ? __builtin_amdgcn_exp2f(mlv[s][0] - M) : 0.f;
    L = fmaf(wt, mlv[s][1], L); o += a[s] * wt;
  }
  const float MK = MASKED * LOG2E;
  if (len < LK && !(M > MK + 200.f)) {
    const float M2 = fmaxf(M, MK);
    const float w1 = (L > 0.f) ? __builtin_amdgcn_exp2f(M - M2) : 0.f, w2 = __builtin_amdgcn_exp2f(MK - M2);
    f32x4 sv = {0.f, 0.f, 0.f, 0.f};
    const int kvh = (row % H) >> 1;
    for (int t = len; t < LK; ++t) {
      const ushort4 r = *reinterpret_cast<const ushort4*>(vc + (size_t)bt[b * NB + (t >> 4)] * PAGE_BYTES + (size_t)(t & 15) * ROW_BYTES + kvh * 512 + lane * 8);
      sv[0] += __uint_as_float((unsigned)r.x << 16); sv[1] += __uint_as_float((unsigned)r.y << 16);
      sv[2] += __uint_as_float((unsigned)r.z << 16); sv[3] += __uint_as_float((unsigned)r.w << 16);
    }
    o = o * w1 + sv * w2; L = L * w1 + (float)(LK - len) * w2;
  }
  const float inv = 1.f / L;
  uint2 r = {cvtpk(o[0] * inv, o[1] * inv), cvtpk(o[2] * inv, o[3] * inv)};
  *reinterpret_cast<uint2*>(out + (size_t)row * D + lane * 4) = r;
}

extern "C" void kernel_launch(void* const* d_in, const int* in_sizes, int n_in, void* d_out, int out_size,
                              void* d_ws, size_t ws_size, hipStream_t stream) {
  attn_split<<<NWG, NW * 64, 0, stream>>>((const char*)d_in[0], (const char*)d_in[1], (const char*)d_in[2],
                                          (const int*)d_in[3], (const int*)d_in[4], (char*)d_ws);
  attn_combine<<<B * H / 4, 256, 0, stream>>>((const char*)d_ws, (const char*)d_in[2], (const int*)d_in[3], (const int*)d_in[4],
                                              (__hip_bfloat16*)d_out);
}
```

```cpp
#include <hip/hip_runtime.h>
#include <hip/hip_bf16.h>
#include <cstdint>

constexpr int B = 32, H = 16, HKV = 8, D = 256, BS = 16, NB = 128, LK = NB * BS;
constexpr int PAGE_BYTES = BS * HKV * D * 2;
constexpr int ROW_BYTES = HKV * D * 2;
constexpr int NW = 8, NWG = 256;
constexpr int NSH = 4, NPR = 3, NSLOT = 8;
constexpr int NPC = 4;
#define CS_NUM 3
#define CS_DEN 2
#define CS_ADD 5
#define CODE_LINES 212
constexpr float LOG2E = 1.4426950408889634f;
constexpr float C2 = 0.0625f * LOG2E;
constexpr float MASKED = -998244352.0f;
constexpr size_t WS_ML = 0, WS_ACC = (size_t)B * H * NSLOT * 2 * 4;

using bf16x8 = __attribute__((ext_vector_type(8))) __bf16;
using u32x4 = __attribute__((ext_vector_type(4))) unsigned;
using f32x4 = __attribute__((ext_vector_type(4))) float;
using f32x2 = __attribute__((ext_vector_type(2))) float;
using bf16x2 = __attribute__((ext_vector_type(2))) __bf16;

template <int NT> __device__ __forceinline__ u32x4 ld16(const char* p) {
  if (NT) return __builtin_nontemporal_load(reinterpret_cast<const u32x4*>(p));
  return *reinterpret_cast<const u32x4*>(p);
}
__device__ __forceinline__ unsigned cvtpk(float lo, float hi) {
  return __builtin_bit_cast(unsigned, __builtin_convertvector(f32x2{lo, hi}, bf16x2));
}
__device__ __forceinline__ float xrow16_max(float x) {
  auto s = __builtin_amdgcn_permlane16_swap(__float_as_uint(x), __float_as_uint(x), false, false);
  x = fmaxf(__uint_as_float(s[0]), __uint_as_float(s[1]));
  auto t = __builtin_amdgcn_permlane32_swap(__float_as_uint(x), __float_as_uint(x), false, false);
  return fmaxf(__uint_as_float(t[0]), __uint_as_float(t[1]));
}
__device__ __forceinline__ float xrow16_sum(float x) {
  auto s = __builtin_amdgcn_permlane16_swap(__float_as_uint(x), __float_as_uint(x), false, false);
  x = __uint_as_float(s[0]) + __uint_as_float(s[1]);
  auto t = __builtin_amdgcn_permlane32_swap(__float_as_uint(x), __float_as_uint(x), false, false);
  return __uint_as_float(t[0]) + __uint_as_float(t[1]);
}

struct Tile { u32x4 k[8]; u32x4 v[8]; };

template <int CTRL> __device__ __forceinline__ int dpp_i(int x, int fill) { return __builtin_amdgcn_update_dpp(fill, x, CTRL, 0xf, 0xf, false); }
template <int CTRL, int RM> __device__ __forceinline__ int dpp_m(int x, int fill) { return __builtin_amdgcn_update_dpp(fill, x, CTRL, RM, 0xf, false); }
__device__ __forceinline__ int scan_add(int x) {
  x += dpp_i<0x111>(x, 0); x += dpp_i<0x112>(x, 0); x += dpp_i<0x114>(x, 0); x += dpp_i<0x118>(x, 0);
  x += dpp_m<0x142, 0xa>(x, 0); x += dpp_m<0x143, 0xc>(x, 0); return x;
}
__device__ __forceinline__ int scan_min(int x) {
  constexpr int F = 0x7fffffff;
  x = min(x, dpp_i<0x111>(x, F)); x = min(x, dpp_i<0x112>(x, F)); x = min(x, dpp_i<0x114>(x, F)); x = min(x, dpp_i<0x118>(x, F));
  x = min(x, dpp_m<0x142, 0xa>(x, F)); x = min(x, dpp_m<0x143, 0xc>(x, F)); return x;
}

template <int NT> __device__ __forceinline__ void issue(Tile& t, const char* kp, const char* vp) {
#pragma unroll
  for (int j = 0; j < 8; ++j) t.k[j] = ld16<NT>(kp + j * 2 * ROW_BYTES);
#pragma unroll
  for (int j = 0; j < 8; ++j) t.v[j] = ld16<NT>(vp + j * 2 * ROW_BYTES);
}

struct LaneC { int wr_off, rd_off, tok0; unsigned pmask; };

__device__ __forceinline__ void kwrite(const u32x4 (&k)[8], char* ks, const LaneC& lc) {
#pragma unroll
  for (int j = 0; j < 8; ++j) *reinterpret_cast<u32x4*>(ks + j * 1024 + (lc.wr_off ^ (j * 16))) = k[j];
}
__device__ __forceinline__ void core(const u32x4 (&v)[8], const u32x4 (&q)[8], int rem, const char* ks, const LaneC& lc,
                                     float& m, float& l, f32x4 (&acc)[8]) {
  f32x4 s0 = {0.f, 0.f, 0.f, 0.f}, s1 = {0.f, 0.f, 0.f, 0.f};
#pragma unroll
  for (int j = 0; j < 4; ++j) {
    const u32x4 ka = *reinterpret_cast<const u32x4*>(ks + (lc.rd_off ^ (2 * j * 64)));
    const u32x4 kb = *reinterpret_cast<const u32x4*>(ks + (lc.rd_off ^ ((2 * j + 1) * 64)));
    s0 = __builtin_amdgcn_mfma_f32_16x16x32_bf16(__builtin_bit_cast(bf16x8, ka), __builtin_bit_cast(bf16x8, q[2 * j]), s0, 0, 0, 0);
    s1 = __builtin_amdgcn_mfma_f32_16x16x32_bf16(__builtin_bit_cast(bf16x8, kb), __builtin_bit_cast(bf16x8, q[2 * j + 1]), s1, 0, 0, 0);
  }
  float x[4];
#pragma unroll
  for (int i = 0; i < 4; ++i) {
    x[i] = (s0[i] + s1[i]) * C2;
    if (rem < BS) x[i] = (lc.tok0 + 2 * i < rem) ? x[i] : -INFINITY;
  }
  float mx = fmaxf(fmaxf(x[0], x[1]), fmaxf(x[2], x[3]));
  mx = xrow16_max(mx);
  const float mn = fmaxf(m, mx);
  const float msub = (mn == -INFINITY) ? 0.f : mn;
  const float alpha = __builtin_amdgcn_exp2f(m - msub);
  float p[4];
#pragma unroll
  for (int i = 0; i < 4; ++i) p[i] = __builtin_amdgcn_exp2f(x[i] - msub);
  l = fmaf(l, alpha, (p[0] + p[1]) + (p[2] + p[3]));
  if (__builtin_amdgcn_ballot_w64(mn != m) != 0) {
#pragma unroll
    for (int u = 0; u < 8; ++u) acc[u] *= alpha;
  }
  m = mn;
  const unsigned p01 = cvtpk(p[0], p[1]), p23 = cvtpk(p[2], p[3]);
  auto sa = __builtin_amdgcn_permlane16_swap(p01, p01, false, false);
  auto sb = __builtin_amdgcn_permlane16_swap(p23, p23, false, false);
  const u32x4 pb = {sa[0] & lc.pmask, sb[0] & lc.pmask, sa[1] & lc.pmask, sb[1] & lc.pmask};
#pragma unroll
  for (int d = 0; d < 8; ++d) {
    const unsigned sel = (d & 1) ? 0x07060302u : 0x05040100u;
    const u32x4 a = {__builtin_amdgcn_perm(v[1][d >> 1], v[0][d >> 1], sel), __builtin_amdgcn_perm(v[3][d >> 1], v[2][d >> 1], sel),
                     __builtin_amdgcn_perm(v[5][d >> 1], v[4][d >> 1], sel), __builtin_amdgcn_perm(v[7][d >> 1], v[6][d >> 1], sel)};
    acc[d] = __builtin_amdgcn_mfma_f32_16x16x32_bf16(__builtin_bit_cast(bf16x8, a), __builtin_bit_cast(bf16x8, pb), acc[d], 0, 0, 0);
  }
}

__global__ void __launch_bounds__(NW * 64, 2)
attn_split(const char* __restrict__ qg, const char* __restrict__ kc, const char* __restrict__ vc,
           const int* __restrict__ bt, const int* __restrict__ lens, char* __restrict__ ws) {
  __shared__ __attribute__((aligned(16))) char big[131072];
  __shared__ __attribute__((aligned(16))) int lds_bt[B * NB];
  __shared__ __attribute__((aligned(16))) char lds_q[NPC][1728];
  __shared__ __attribute__((aligned(16))) int lds_min[NW];
  __shared__ float lds_ml[NPC][NW][2][2];
  const int wg = blockIdx.x, tid = threadIdx.x, lane = tid & 63, wave = __builtin_amdgcn_readfirstlane(tid >> 6);
  const int g = lane >> 4, c = lane & 15;
#define STAMP(I)
  STAMP(0);
  float* ws_ml = reinterpret_cast<float*>(ws + WS_ML);
  float* ws_acc = reinterpret_cast<float*>(ws + WS_ACC);
  int len = lens[lane & 31];
  unsigned pf = 0;
  if (tid < CODE_LINES) {
    unsigned long long pc; asm volatile("s_getpc_b64 %0" : "=s"(pc));
    pf = *reinterpret_cast<const unsigned*>((pc & ~255ull) + tid * 64);
  }
  {
    const int col = (tid & 15) * 8;
    const u32x4 a0 = *reinterpret_cast<const u32x4*>(bt + tid * 8), a1 = *reinterpret_cast<const u32x4*>(bt + tid * 8 + 4);
    const u32x4 r0 = *reinterpret_cast<const u32x4*>(bt + col), r1 = *reinterpret_cast<const u32x4*>(bt + col + 4);
    *reinterpret_cast<u32x4*>(lds_bt + tid * 8) = a0; *reinterpret_cast<u32x4*>(lds_bt + tid * 8 + 4) = a1;
    int fm = NB;
#pragma unroll
    for (int e = 3; e >= 0; --e) if (a1[e] != r1[e]) fm = col + 4 + e;
#pragma unroll
    for (int e = 3; e >= 0; --e) if (a0[e] != r0[e]) fm = col + e;
    fm = scan_min(fm);
    if (lane == 63) lds_min[wave] = fm;
    if (wave < NPC && lane < 32) *reinterpret_cast<u32x4*>(lds_q[wave] + 2 * 576 + lane * 16) = u32x4{0u, 0u, 0u, 0u};
  }
  asm volatile("" :: "v"(pf));
  len = min(max(len, 0), LK);
  const int npg = (len + BS - 1) / BS;
  const int minlen = __builtin_amdgcn_readlane(scan_min(len), 31), mxn = -__builtin_amdgcn_readlane(scan_min(-npg), 31);
  const int incn = scan_add(npg);
  __syncthreads();
  int S = minlen / BS;
  {
    const int4 m0 = *reinterpret_cast<const int4*>(lds_min), m1 = *reinterpret_cast<const int4*>(lds_min + 4);
    S = min(min(min(S, m0.x), min(m0.y, m0.z)), min(min(m0.w, m1.x), min(min(m1.y, m1.z), m1.w)));
  }
  S = __builtin_amdgcn_readfirstlane(S);
  STAMP(1);
  const int priv = npg - S;
  const int incl = incn - S * (lane + 1);
  const int Tp = 8 * __builtin_amdgcn_readlane(incl, 31), mxp = mxn - S;
  const int Ns = S > 0 ? 8 * NSH : 0, per = (S + NSH - 1) / NSH, cs = S > 0 ? per * CS_NUM / CS_DEN + CS_ADD : 0;
  const int umin = max((mxp + NPR - 2) / (NPR - 1), 1);
  int U = max((Tp + Ns * cs + NWG - 1) / NWG, umin), Us = U - cs;
  if (Us < umin) { Us = 0; U = max((Tp + (NWG - Ns) - 1) / (NWG - Ns), umin); }
  const int usplit = Ns * Us;
  auto wg_of = [&](int u) { return u < usplit ? u / Us : Ns + (u - usplit) / U; };
  LaneC lc;
  lc.wr_off = (lane >> 5) * 512 + (((lane & 31) ^ (8 * (lane >> 5))) * 16);
  {
    const int tk = 8 * ((c >> 2) & 1) + 2 * (c & 3) + (c >> 3);
    lc.rd_off = tk * 512 + ((g ^ c) * 16);
  }
  lc.tok0 = 8 * (g & 1) + (g >> 1);
  lc.pmask = ((g & 1) == (c & 1)) ? 0xffffffffu : 0u;
  const int loff = (lane >> 5) * ROW_BYTES + (lane & 31) * 16;
  const char* kl = kc + loff;
  const char* vl = vc + loff;
  u32x4 q[8];
  float m, l;
  f32x4 acc[8];
  Tile ta, tb;
  if (wg < Ns) {
    const int kvh = wg & 7, sp = wg >> 3;
    const int p0 = min(sp * per, S), n = min(p0 + per, S) - p0;
    const int qr = c >> 1, row = (4 * wave + (qr >> 1)) * H + 2 * kvh + (qr & 1);
    if (n > 0) { const size_t pb_ = (size_t)lds_bt[p0 + min(wave, n - 1)] * PAGE_BYTES + kvh * 512; issue<1>(ta, kl + pb_, vl + pb_); }
#pragma unroll
    for (int j = 0; j < 8; ++j) q[j] = *reinterpret_cast<const u32x4*>(qg + (size_t)row * (D * 2) + (4 * j + g) * 16);
    m = -INFINITY; l = 0.f;
#pragma unroll
    for (int u = 0; u < 8; ++u) acc[u] = f32x4{0.f, 0.f, 0.f, 0.f};
    for (int r = 0; r < n; r += NW) {
      char* st = big + wave * 16384;
      kwrite(ta.k, st, lc);
#pragma unroll
      for (int j = 0; j < 8; ++j) *reinterpret_cast<u32x4*>(st + 8192 + j * 1024 + lane * 16) = ta.v[j];
      { const size_t pb_ = (size_t)lds_bt[p0 + min(r + NW + wave, n - 1)] * PAGE_BYTES + kvh * 512; if (r + NW < n) issue<1>(ta, kl + pb_, vl + pb_); }
      __syncthreads();
      const int cnt = min(NW, n - r);
      for (int pg = 0; pg < cnt; ++pg) {
        const char* sp_ = big + pg * 16384;
#pragma unroll
        for (int j = 0; j < 8; ++j) tb.v[j] = *reinterpret_cast<const u32x4*>(sp_ + 8192 + j * 1024 + lane * 16);
        core(tb.v, q, BS, sp_, lc, m, l, acc);
      }
      __syncthreads();
    }
    l = xrow16_sum(l);
    const size_t rs = (size_t)row * NSLOT + NPR + sp;
#pragma unroll
    for (int i = 0; i < 4; ++i) {
      float* dst = ws_acc + rs * D + 128 * (c & 1) + 32 * g + 8 * i;
      *reinterpret_cast<f32x4*>(dst) = f32x4{acc[0][i], acc[1][i], acc[2][i], acc[3][i]};
      *reinterpret_cast<f32x4*>(dst + 4) = f32x4{acc[4][i], acc[5][i], acc[6][i], acc[7][i]};
    }
    if (g == 0 && (c & 1) == 0) *reinterpret_cast<f32x2*>(ws_ml + rs * 2) = f32x2{m, l};
  }
  STAMP(2);
  char* ks = big + wave * 8192;
  const int qoff = min(c >> 1, 2) * 576 + g * 16;
  const char* qs = lds_q[0];
  int u = wg < Ns ? wg * Us : usplit + (wg - Ns) * U;
  const int u1 = min(u + (wg < Ns ? Us : U), Tp);
  bool first = true;
  while (u < u1) {
    const int uu = u + wave + NW * lane;
    int offv = 0, remv = BS, pcv = NPC, ue = u, rowp = 0, slotp = 0, npcs = 0, qrow = 0;
#pragma unroll
    for (int p = 0; p < NPC; ++p) {
      if (ue < u1) {
        const int b = __builtin_ctzll(__builtin_amdgcn_ballot_w64(lane < 32 && 8 * incl > ue));
        const int pb = __builtin_amdgcn_readlane(priv, b), st = 8 * (__builtin_amdgcn_readlane(incl, b) - pb);
        const int lb = __builtin_amdgcn_readlane(len, b);
        const int off = ue - st, kvh = off / pb, pg0 = off - kvh * pb, np = min(pb - pg0, u1 - ue);
        if (uu >= ue && uu < ue + np) {
          const int lp = S + pg0 + (uu - ue);
          offv = lds_bt[b * NB + lp] * PAGE_BYTES + kvh * 512; remv = lb - lp * BS; pcv = p;
        }
        if (wave == p) qrow = b * H + 2 * kvh;
        if ((tid >> 7) == p) { rowp = b * H + 2 * kvh; slotp = wg - wg_of(st + kvh * pb); }
        ue += np; npcs = p + 1;
      }
    }
    const u32x4 qst = *reinterpret_cast<const u32x4*>(qg + (size_t)qrow * (D * 2) + lane * 16);
    const int n = (ue - u > wave) ? (ue - u - wave + NW - 1) / NW : 0;
    if (lane < NPC * 2) { lds_ml[lane >> 1][wave][lane & 1][0] = -INFINITY; lds_ml[lane >> 1][wave][lane & 1][1] = 0.f; }
#define ISSUE(T, I) do { const unsigned pb_ = (unsigned)__builtin_amdgcn_readlane(offv, (I)); issue<1>(T, kl + pb_, vl + pb_); } while (0)
#define LOADQ(P) do { qs = lds_q[P] + qoff; m = -INFINITY; l = 0.f; _Pragma("unroll") for (int v = 0; v < 8; ++v) acc[v] = f32x4{0.f, 0.f, 0.f, 0.f}; } while (0)
#define FLUSH(P) do { l = xrow16_sum(l); if (c < 4) { \
      float* mrg = reinterpret_cast<float*>(big + 65536 + ((P) * NW + wave) * 2048) + (c >> 1) * D + 128 * (c & 1) + 32 * g; \
      _Pragma("unroll") for (int i = 0; i < 4; ++i) { \
        *reinterpret_cast<f32x4*>(mrg + 8 * i) = f32x4{acc[0][i], acc[1][i], acc[2][i], acc[3][i]}; \
        *reinterpret_cast<f32x4*>(mrg + 8 * i + 4) = f32x4{acc[4][i], acc[5][i], acc[6][i], acc[7][i]}; } \
      if (g == 0 && (c & 1) == 0) { lds_ml[P][wave][c >> 1][0] = m; lds_ml[P][wave][c >> 1][1] = l; } } } while (0)
#define COMPUTE
    if (n > 0) ISSUE(ta, 0);
    if (wave < npcs) *reinterpret_cast<u32x4*>(lds_q[wave] + (lane >> 5) * 576 + (lane & 31) * 16) = qst;
    if (first) { first = false;
      {
        const int b = wg >> 3, kvh = wg & 7;
        const int pb = __builtin_amdgcn_readlane(priv, b), st = 8 * (__builtin_amdgcn_readlane(incl, b) - pb) + kvh * pb;
        const int npc = pb > 0 ? wg_of(st + pb - 1) - wg_of(st) + 1 : 0;
        const int slot = wave;
        if ((slot < NPR && slot >= npc) || (slot >= NPR && slot < NPR + NSH && Ns == 0)) {
    #pragma unroll
          for (int h = 0; h < 2; ++h) {
            const size_t rs = (size_t)(b * H + 2 * kvh + h) * NSLOT + slot;
            *reinterpret_cast<f32x4*>(ws_acc + rs * D + lane * 4) = f32x4{0.f, 0.f, 0.f, 0.f};
            if (lane == 0) *reinterpret_cast<f32x2*>(ws_ml + rs * 2) = f32x2{-INFINITY, 0.f};
          }
        }
      }
     }
    __syncthreads();
    STAMP(3);
    if (n > 0) {
      int pc = __builtin_amdgcn_readlane(pcv, 0);
      LOADQ(pc);
      for (int i = 0; i < n; ++i) {
        kwrite(ta.k, ks, lc);
#pragma unroll
        for (int j = 0; j < 8; ++j) tb.v[j] = ta.v[j];
        if (i + 1 < n) ISSUE(ta, i + 1);
#pragma unroll
        for (int j = 0; j < 8; ++j) q[j] = *reinterpret_cast<const u32x4*>(qs + j * 64);
        core(tb.v, q, __builtin_amdgcn_readlane(remv, i), ks, lc, m, l, acc);
        const int pn = __builtin_amdgcn_readlane(pcv, i + 1);
        if (pn != pc) { FLUSH(pc); pc = pn; if (pc < NPC) LOADQ(pc); }
      }
    }
#undef ISSUE
#undef LOADQ
#undef FLUSH
#undef COMPUTE
    STAMP(4);
    __syncthreads();
    STAMP(5);
    {
      const int p = tid >> 7, h = (tid >> 6) & 1;
      if (p < npcs) {
        float M = -INFINITY;
#pragma unroll
        for (int w = 0; w < NW; ++w) M = fmaxf(M, lds_ml[p][w][h][0]);
        float L = 0.f; f32x4 o = {0.f, 0.f, 0.f, 0.f};
#pragma unroll
        for (int w = 0; w < NW; ++w) {
          const float lw = lds_ml[p][w][h][1];
          if (lw > 0.f) {
            const float wt = __builtin_amdgcn_exp2f(lds_ml[p][w][h][0] - M);
            L = fmaf(wt, lw, L); o += *reinterpret_cast<const f32x4*>(big + 65536 + (p * NW + w) * 2048 + h * 1024 + lane * 16) * wt;
          }
        }
        const size_t rs = (size_t)(rowp + h) * NSLOT + slotp;
        *reinterpret_cast<f32x4*>(ws_acc + rs * D + lane * 4) = o;
        if (lane == 0) *reinterpret_cast<f32x2*>(ws_ml + rs * 2) = f32x2{M, L};
      }
    }
    u = ue;
    if (u < u1) __syncthreads();
  }
  if (first) {
  {
    const int b = wg >> 3, kvh = wg & 7;
    const int pb = __builtin_amdgcn_readlane(priv, b), st = 8 * (__builtin_amdgcn_readlane(incl, b) - pb) + kvh * pb;
    const int npc = pb > 0 ? wg_of(st + pb - 1) - wg_of(st) + 1 : 0;
    const int slot = wave;
    if ((slot < NPR && slot >= npc) || (slot >= NPR && slot < NPR + NSH && Ns == 0)) {
#pragma unroll
      for (int h = 0; h < 2; ++h) {
        const size_t rs = (size_t)(b * H + 2 * kvh + h) * NSLOT + slot;
        *reinterpret_cast<f32x4*>(ws_acc + rs * D + lane * 4) = f32x4{0.f, 0.f, 0.f, 0.f};
        if (lane == 0) *reinterpret_cast<f32x2*>(ws_ml + rs * 2) = f32x2{-INFINITY, 0.f};
      }
    }
  }
  }
  STAMP(6);
}

__global__ void __launch_bounds__(256)
attn_combine(const char* __restrict__ ws, const char* __restrict__ vc, const int* __restrict__ bt, const int* __restrict__ lens,
             __hip_bfloat16* __restrict__ out) {
  const int lane = threadIdx.x & 63, row = blockIdx.x * 4 + (threadIdx.x >> 6);
  const float* ml = reinterpret_cast<const float*>(ws + WS_ML) + (size_t)row * NSLOT * 2;
  const float* ac = reinterpret_cast<const float*>(ws + WS_ACC) + (size_t)row * NSLOT * D + lane * 4;
  f32x2 mlv[7]; f32x4 a[7];
#pragma unroll
  for (int s = 0; s < 7; ++s) { mlv[s] = *reinterpret_cast<const f32x2*>(ml + 2 * s); a[s] = *reinterpret_cast<const f32x4*>(ac + s * D); }
  const int b = row / H;
  const int len = min(max(lens[b], 0), LK);
  float M = -INFINITY;
#pragma unroll
  for (int s = 0; s < 7; ++s) M = fmaxf(M, mlv[s][0]);
  float L = 0.f; f32x4 o = {0.f, 0.f, 0.f, 0.f};
#pragma unroll
  for (int s = 0; s < 7; ++s) {
    const float wt = (mlv[s][1] > 0.f) ? __builtin_amdgcn_exp2f(mlv[s][0] - M) : 0.f;
    L = fmaf(wt, mlv[s][1], L); o += a[s] * wt;
  }
  const float MK = MASKED * LOG2E;
  if (len < LK && !(M > MK + 200.f)) {
    const float M2 = fmaxf(M, MK);
    const float w1 = (L > 0.f) ? __builtin_amdgcn_exp2f(M - M2) : 0.f, w2 = __builtin_amdgcn_exp2f(MK - M2);
    f32x4 sv = {0.f, 0.f, 0.f, 0.f};
    const int kvh = (row % H) >> 1;
    for (int t = len; t < LK; ++t) {
      const ushort4 r = *reinterpret_cast<const ushort4*>(vc + (size_t)bt[b * NB + (t >> 4)] * PAGE_BYTES + (size_t)(t & 15) * ROW_BYTES + kvh * 512 + lane * 8);
      sv[0] += __uint_as_float((unsigned)r.x << 16); sv[1] += __uint_as_float((unsigned)r.y << 16);
      sv[2] += __uint_as_float((unsigned)r.z << 16); sv[3] += __uint_as_float((unsigned)r.w << 16);
    }
    o = o * w1 + sv * w2; L = L * w1 + (float)(LK - len) * w2;
  }
  const float inv = 1.f / L;
  uint2 r = {cvtpk(o[0] * inv, o[1] * inv), cvtpk(o[2] * inv, o[3] * inv)};
  *reinterpret_cast<uint2*>(out + (size_t)row * D + lane * 4) = r;
}

extern "C" void kernel_launch(void* const* d_in, const int* in_sizes, int n_in, void* d_out, int out_size,
                              void* d_ws, size_t ws_size, hipStream_t stream) {
  attn_split<<<NWG, NW * 64, 0, stream>>>((const char*)d_in[0], (const char*)d_in[1], (const char*)d_in[2],
                                          (const int*)d_in[3], (const int*)d_in[4], (char*)d_ws);
  attn_combine<<<B * H / 4, 256, 0, stream>>>((const char*)d_ws, (const char*)d_in[2], (const int*)d_in[3], (const int*)d_in[4],
                                              (__hip_bfloat16*)d_out);
}
```

```cpp
#include <hip/hip_runtime.h>
#include <hip/hip_bf16.h>
#include <cstdint>

constexpr int B = 32, H = 16, HKV = 8, D = 256, BS = 16, NB = 128, LK = NB * BS;
constexpr int PAGE_BYTES = BS * HKV * D * 2;
constexpr int ROW_BYTES = HKV * D * 2;
constexpr int NW = 8, NWG = 256;
#define NSHV 4
constexpr int NSH = NSHV, NPR = 3, NSLOT = 8;
constexpr int NPC = 4;
#define CS_NUM 3
#define CS_DEN 2
#define CS_ADD 5
#define CODE_LINES 208
#define NACT 5
constexpr float LOG2E = 1.4426950408889634f;
constexpr float C2 = 0.0625f * LOG2E;
constexpr float MASKED = -998244352.0f;
constexpr size_t WS_ML = 0, WS_ACC = (size_t)B * H * NSLOT * 2 * 4;

using bf16x8 = __attribute__((ext_vector_type(8))) __bf16;
using u32x4 = __attribute__((ext_vector_type(4))) unsigned;
using f32x4 = __attribute__((ext_vector_type(4))) float;
using f32x2 = __attribute__((ext_vector_type(2))) float;
using bf16x2 = __attribute__((ext_vector_type(2))) __bf16;

template <int NT> __device__ __forceinline__ u32x4 ld16(const char* p) {
  if (NT) return __builtin_nontemporal_load(reinterpret_cast<const u32x4*>(p));
  return *reinterpret_cast<const u32x4*>(p);
}
#define NTS 0
template <typename T> __device__ __forceinline__ void stw(float* p, T v) {
  if (NTS & 1) __builtin_nontemporal_store(v, reinterpret_cast<T*>(p)); else *reinterpret_cast<T*>(p) = v;
}
template <typename T> __device__ __forceinline__ T ldw(const float* p) {
  if (NTS & 2) return __builtin_nontemporal_load(reinterpret_cast<const T*>(p)); else return *reinterpret_cast<const T*>(p);
}
__device__ __forceinline__ unsigned cvtpk(float lo, float hi) {
  return __builtin_bit_cast(unsigned, __builtin_convertvector(f32x2{lo, hi}, bf16x2));
}
__device__ __forceinline__ float xrow16_max(float x) {
  auto s = __builtin_amdgcn_permlane16_swap(__float_as_uint(x), __float_as_uint(x), false, false);
  x = fmaxf(__uint_as_float(s[0]), __uint_as_float(s[1]));
  auto t = __builtin_amdgcn_permlane32_swap(__float_as_uint(x), __float_as_uint(x), false, false);
  return fmaxf(__uint_as_float(t[0]), __uint_as_float(t[1]));
}
__device__ __forceinline__ float xrow16_sum(float x) {
  auto s = __builtin_amdgcn_permlane16_swap(__float_as_uint(x), __float_as_uint(x), false, false);
  x = __uint_as_float(s[0]) + __uint_as_float(s[1]);
  auto t = __builtin_amdgcn_permlane32_swap(__float_as_uint(x), __float_as_uint(x), false, false);
  return __uint_as_float(t[0]) + __uint_as_float(t[1]);
}

struct Tile { u32x4 k[8]; u32x4 v[8]; };

template <int CTRL> __device__ __forceinline__ int dpp_i(int x, int fill) { return __builtin_amdgcn_update_dpp(fill, x, CTRL, 0xf, 0xf, false); }
template <int CTRL, int RM> __device__ __forceinline__ int dpp_m(int x, int fill) { return __builtin_amdgcn_update_dpp(fill, x, CTRL, RM, 0xf, false); }
__device__ __forceinline__ int scan_add(int x) {
  x += dpp_i<0x111>(x, 0); x += dpp_i<0x112>(x, 0); x += dpp_i<0x114>(x, 0); x += dpp_i<0x118>(x, 0);
  x += dpp_m<0x142, 0xa>(x, 0); x += dpp_m<0x143, 0xc>(x, 0); return x;
}
__device__ __forceinline__ int scan_min(int x) {
  constexpr int F = 0x7fffffff;
  x = min(x, dpp_i<0x111>(x, F)); x = min(x, dpp_i<0x112>(x, F)); x = min(x, dpp_i<0x114>(x, F)); x = min(x, dpp_i<0x118>(x, F));
  x = min(x, dpp_m<0x142, 0xa>(x, F)); x = min(x, dpp_m<0x143, 0xc>(x, F)); return x;
}

template <int NT> __device__ __forceinline__ void issue(Tile& t, const char* kp, const char* vp) {
#pragma unroll
  for (int j = 0; j < 8; ++j) t.k[j] = ld16<NT>(kp + j * 2 * ROW_BYTES);
#pragma unroll
  for (int j = 0; j < 8; ++j) t.v[j] = ld16<NT>(vp + j * 2 * ROW_BYTES);
}

__device__ __forceinline__ void issue_c(Tile& t, const char* kp, const char* vp, int lane, int rl) {
  unsigned o[8];
#pragma unroll
  for (int j = 0; j < 8; ++j) o[j] = (unsigned)min(2 * j + (lane >> 5), rl) * ROW_BYTES + (lane & 31) * 16;
#pragma unroll
  for (int j = 0; j < 8; ++j) t.k[j] = ld16<1>(kp + o[j]);
#pragma unroll
  for (int j = 0; j < 8; ++j) t.v[j] = ld16<1>(vp + o[j]);
}

struct LaneC { int wr_off, rd_off, tok0; unsigned pmask; };

__device__ __forceinline__ void kwrite(const u32x4 (&k)[8], char* ks, const LaneC& lc) {
#pragma unroll
  for (int j = 0; j < 8; ++j) *reinterpret_cast<u32x4*>(ks + j * 1024 + (lc.wr_off ^ (j * 16))) = k[j];
}
__device__ __forceinline__ u32x4 vop(const u32x4 (&v)[8], int d) {
  const unsigned sel = (d & 1) ? 0x07060302u : 0x05040100u;
  return u32x4{__builtin_amdgcn_perm(v[1][d >> 1], v[0][d >> 1], sel), __builtin_amdgcn_perm(v[3][d >> 1], v[2][d >> 1], sel),
               __builtin_amdgcn_perm(v[5][d >> 1], v[4][d >> 1], sel), __builtin_amdgcn_perm(v[7][d >> 1], v[6][d >> 1], sel)};
}
template <bool PT>
__device__ __forceinline__ void core(const u32x4 (&v)[8], const u32x4 (&q)[8], int rem, const char* ks, const LaneC& lc,
                                     float& m, float& l, f32x4 (&acc)[8]) {
  f32x4 s0 = {0.f, 0.f, 0.f, 0.f}, s1 = {0.f, 0.f, 0.f, 0.f};
#pragma unroll
  for (int j = 0; j < 4; ++j) {
    const u32x4 ka = *reinterpret_cast<const u32x4*>(ks + (lc.rd_off ^ (2 * j * 64)));
    const u32x4 kb = *reinterpret_cast<const u32x4*>(ks + (lc.rd_off ^ ((2 * j + 1) * 64)));
    s0 = __builtin_amdgcn_mfma_f32_16x16x32_bf16(__builtin_bit_cast(bf16x8, ka), __builtin_bit_cast(bf16x8, q[2 * j]), s0, 0, 0, 0);
    s1 = __builtin_amdgcn_mfma_f32_16x16x32_bf16(__builtin_bit_cast(bf16x8, kb), __builtin_bit_cast(bf16x8, q[2 * j + 1]), s1, 0, 0, 0);
  }
  float x[4];
#pragma unroll
  for (int i = 0; i < 4; ++i) {
    x[i] = (s0[i] + s1[i]) * C2;
    if (rem < BS) x[i] = (lc.tok0 + 2 * i < rem) ? x[i] : -INFINITY;
  }
  float mx = fmaxf(fmaxf(x[0], x[1]), fmaxf(x[2], x[3]));
  mx = xrow16_max(mx);
  const float mn = fmaxf(m, mx);
  const float msub = (mn == -INFINITY) ? 0.f : mn;
  const float alpha = __builtin_amdgcn_exp2f(m - msub);
  float p[4];
#pragma unroll
  for (int i = 0; i < 4; ++i) p[i] = __builtin_amdgcn_exp2f(x[i] - msub);
  l = fmaf(l, alpha, (p[0] + p[1]) + (p[2] + p[3]));
  if (__builtin_amdgcn_ballot_w64(mn != m) != 0) {
#pragma unroll
    for (int u = 0; u < 8; ++u) acc[u] *= alpha;
  }
  m = mn;
  const unsigned p01 = cvtpk(p[0], p[1]), p23 = cvtpk(p[2], p[3]);
  auto sa = __builtin_amdgcn_permlane16_swap(p01, p01, false, false);
  auto sb = __builtin_amdgcn_permlane16_swap(p23, p23, false, false);
  const u32x4 pb = {sa[0] & lc.pmask, sb[0] & lc.pmask, sa[1] & lc.pmask, sb[1] & lc.pmask};
#pragma unroll
  for (int d = 0; d < 8; ++d) {
    const u32x4 a = PT ? v[d] : vop(v, d);
    acc[d] = __builtin_amdgcn_mfma_f32_16x16x32_bf16(__builtin_bit_cast(bf16x8, a), __builtin_bit_cast(bf16x8, pb), acc[d], 0, 0, 0);
  }
}

__global__ void __launch_bounds__(NW * 64, 2)
attn_split(const char* __restrict__ qg, const char* __restrict__ kc, const char* __restrict__ vc,
           const int* __restrict__ bt, const int* __restrict__ lens, char* __restrict__ ws) {
  __shared__ __attribute__((aligned(16))) char big[131072];
  __shared__ __attribute__((aligned(16))) int lds_bt[B * NB];
  __shared__ __attribute__((aligned(16))) char lds_q[NPC][1728];
  __shared__ __attribute__((aligned(16))) int lds_min[NW];
  __shared__ __attribute__((aligned(8))) float lds_ml[NPC][NW][2][2];
  __shared__ uint2 lds_desc[NW * 64];
  __shared__ int lds_ctr;
  const int wg = blockIdx.x, tid = threadIdx.x, lane = tid & 63, wave = __builtin_amdgcn_readfirstlane(tid >> 6);
  const int g = lane >> 4, c = lane & 15;
#define STAMP(I)
  STAMP(0);
  float* ws_ml = reinterpret_cast<float*>(ws + WS_ML);
  float* ws_acc = reinterpret_cast<float*>(ws + WS_ACC);
  int len = lens[lane & 31];
  unsigned pf = 0;
  if (tid < CODE_LINES) {
    unsigned long long pc; asm volatile("s_getpc_b64 %0" : "=s"(pc));
    pf = *reinterpret_cast<const unsigned*>((pc & ~255ull) + tid * 64);
  }
  {
    const int col = (tid & 15) * 8;
    const u32x4 a0 = *reinterpret_cast<const u32x4*>(bt + tid * 8), a1 = *reinterpret_cast<const u32x4*>(bt + tid * 8 + 4);
    const u32x4 r0 = *reinterpret_cast<const u32x4*>(bt + col), r1 = *reinterpret_cast<const u32x4*>(bt + col + 4);
    *reinterpret_cast<u32x4*>(lds_bt + tid * 8) = a0; *reinterpret_cast<u32x4*>(lds_bt + tid * 8 + 4) = a1;
    int fm = NB;
#pragma unroll
    for (int e = 3; e >= 0; --e) if (a1[e] != r1[e]) fm = col + 4 + e;
#pragma unroll
    for (int e = 3; e >= 0; --e) if (a0[e] != r0[e]) fm = col + e;
    fm = scan_min(fm);
    if (lane == 63) lds_min[wave] = fm;
    if (wave < NPC && lane < 32) *reinterpret_cast<u32x4*>(lds_q[wave] + 2 * 576 + lane * 16) = u32x4{0u, 0u, 0u, 0u};
  }
  asm volatile("" :: "v"(pf));
  len = min(max(len, 0), LK);
  const int npg = (len + BS - 1) / BS;
  const int minlen = __builtin_amdgcn_readlane(scan_min(len), 31), mxn = -__builtin_amdgcn_readlane(scan_min(-npg), 31);
  const int incn = scan_add(npg);
  __syncthreads();
  int S = minlen / BS;
  {
    const int4 m0 = *reinterpret_cast<const int4*>(lds_min), m1 = *reinterpret_cast<const int4*>(lds_min + 4);
    S = min(min(min(S, m0.x), min(m0.y, m0.z)), min(min(m0.w, m1.x), min(min(m1.y, m1.z), m1.w)));
  }
  S = __builtin_amdgcn_readfirstlane(S);
  STAMP(1);
  const int priv = npg - S;
  const int incl = incn - S * (lane + 1);
  const int Tp = 8 * __builtin_amdgcn_readlane(incl, 31), mxp = mxn - S;
  const int Ns = S > 0 ? 8 * NSH : 0, per = (S + NSH - 1) / NSH, cs = S > 0 ? per * CS_NUM / CS_DEN + CS_ADD : 0;
  const int umin = max((mxp + NPR - 2) / (NPR - 1), 1);
  int U = max((Tp + Ns * cs + NWG - 1) / NWG, umin), Us = U - cs;
  if (Us < umin) { Us = 0; U = max(Ns ? (Tp + (NWG - 8 * NSH) - 1) / (NWG - 8 * NSH) : (Tp + NWG - 1) / NWG, umin); }
  const int usplit = Ns * Us;
  auto wg_of = [&](int u) { return u < usplit ? u / Us : Ns + (u - usplit) / U; };
  LaneC lc;
  lc.wr_off = (lane >> 5) * 512 + (((lane & 31) ^ (8 * (lane >> 5))) * 16);
  {
    const int tk = 8 * ((c >> 2) & 1) + 2 * (c & 3) + (c >> 3);
    lc.rd_off = tk * 512 + ((g ^ c) * 16);
  }
  lc.tok0 = 8 * (g & 1) + (g >> 1);
  lc.pmask = ((g & 1) == (c & 1)) ? 0xffffffffu : 0u;
  const int loff = (lane >> 5) * ROW_BYTES + (lane & 31) * 16;
  const char* kl = kc + loff;
  const char* vl = vc + loff;
  u32x4 q[8];
  float m, l;
  f32x4 acc[8];
  Tile ta, tb;
  if (wg < Ns) {
    const int kvh = wg & 7, sp = wg >> 3;
    const int p0 = min(sp * per, S), n = min(p0 + per, S) - p0;
    const int qr = c >> 1, row = (4 * wave + (qr >> 1)) * H + 2 * kvh + (qr & 1);
    if (n > 0) { const size_t pb_ = (size_t)lds_bt[p0 + min(wave, n - 1)] * PAGE_BYTES + kvh * 512; issue<1>(ta, kl + pb_, vl + pb_); }
#pragma unroll
    for (int j = 0; j < 8; ++j) q[j] = *reinterpret_cast<const u32x4*>(qg + (size_t)row * (D * 2) + (4 * j + g) * 16);
    m = -INFINITY; l = 0.f;
#pragma unroll
    for (int u = 0; u < 8; ++u) acc[u] = f32x4{0.f, 0.f, 0.f, 0.f};
    for (int r = 0; r < n; r += NW) {
      char* st = big + wave * 16384;
      kwrite(ta.k, st, lc);
#pragma unroll
      for (int j = 0; j < 8; ++j) *reinterpret_cast<u32x4*>(st + 8192 + j * 1024 + lane * 16) = vop(ta.v, j);
      { const size_t pb_ = (size_t)lds_bt[p0 + min(r + NW + wave, n - 1)] * PAGE_BYTES + kvh * 512; if (r + NW < n) issue<1>(ta, kl + pb_, vl + pb_); }
      __syncthreads();
      const int cnt = min(NW, n - r);
      for (int pg = 0; pg < cnt; ++pg) {
        const char* sp_ = big + pg * 16384;
#pragma unroll
        for (int j = 0; j < 8; ++j) tb.v[j] = *reinterpret_cast<const u32x4*>(sp_ + 8192 + j * 1024 + lane * 16);
        core<true>(tb.v, q, BS, sp_, lc, m, l, acc);
      }
      __syncthreads();
    }
    l = xrow16_sum(l);
    const size_t rs = (size_t)row * NSLOT + NPR + sp;
    {
      float* tr = reinterpret_cast<float*>(big + wave * 16384);
#pragma unroll
      for (int i = 0; i < 4; ++i) {
        float* dst = tr + qr * (D + 4) + 128 * (c & 1) + 32 * g + 8 * i;
        *reinterpret_cast<f32x4*>(dst) = f32x4{acc[0][i], acc[1][i], acc[2][i], acc[3][i]};
        *reinterpret_cast<f32x4*>(dst + 4) = f32x4{acc[4][i], acc[5][i], acc[6][i], acc[7][i]};
      }
      __builtin_amdgcn_wave_barrier();
#pragma unroll
      for (int r = 0; r < 8; ++r) {
        const size_t rr = (size_t)((4 * wave + (r >> 1)) * H + 2 * kvh + (r & 1)) * NSLOT + NPR + sp;
        stw(ws_acc + rr * D + lane * 4, *reinterpret_cast<const f32x4*>(tr + r * (D + 4) + lane * 4));
      }
      __builtin_amdgcn_wave_barrier();
    }
    if (g == 0 && (c & 1) == 0) *reinterpret_cast<f32x2*>(ws_ml + rs * 2) = f32x2{m, l};
  }
  STAMP(2);
  char* ks = big + wave * 8192;
  const int qoff = min(c >> 1, 2) * 576 + g * 16;
  const char* qs = lds_q[0];
  int u = wg < Ns ? wg * Us : usplit + (wg - Ns) * U;
  const int u1 = min(u + (wg < Ns ? Us : U), Tp);
  bool first = true;
  while (u < u1) {
    const int uu = u + wave + NW * lane;
    int offv = 0, remv = BS, pcv = NPC, ue = u, rowp = 0, slotp = 0, npcs = 0, qrow = 0;
#pragma unroll
    for (int p = 0; p < NPC; ++p) {
      if (ue < u1) {
        const int b = __builtin_ctzll(__builtin_amdgcn_ballot_w64(lane < 32 && 8 * incl > ue));
        const int pb = __builtin_amdgcn_readlane(priv, b), st = 8 * (__builtin_amdgcn_readlane(incl, b) - pb);
        const int lb = __builtin_amdgcn_readlane(len, b);
        const int off = ue - st, kvh = __builtin_popcountll(__builtin_amdgcn_ballot_w64(lane < 7 && (lane + 1) * pb <= off)), pg0 = off - kvh * pb, np = min(pb - pg0, u1 - ue);
        if (uu >= ue && uu < ue + np) {
          const int lp = S + pg0 + (uu - ue);
          offv = lds_bt[b * NB + lp] * PAGE_BYTES + kvh * 512; remv = lb - lp * BS; pcv = p;
        }
        if (wave == p) qrow = b * H + 2 * kvh;
        if ((tid >> 7) == p) { rowp = b * H + 2 * kvh; slotp = st + kvh * pb; }
        ue += np; npcs = p + 1;
      }
    }
    const u32x4 qst = *reinterpret_cast<const u32x4*>(qg + (size_t)qrow * (D * 2) + lane * 16);
    const int cnt = ue - u;
    const int metav = min(remv, BS) | (pcv << 8);
    if (lane < NPC * 2) { lds_ml[lane >> 1][wave][lane & 1][0] = -INFINITY; lds_ml[lane >> 1][wave][lane & 1][1] = 0.f; }
#define LOADQ(P) do { qs = lds_q[P] + qoff; m = -INFINITY; l = 0.f; _Pragma("unroll") for (int v = 0; v < 8; ++v) acc[v] = f32x4{0.f, 0.f, 0.f, 0.f}; } while (0)
#define FLUSH(P) do { l = xrow16_sum(l); if (c < 4) { \
      float* mrg = reinterpret_cast<float*>(big + 65536 + ((P) * NW + wave) * 2048) + (c >> 1) * D + 128 * (c & 1) + 32 * g; \
      _Pragma("unroll") for (int i = 0; i < 4; ++i) { \
        *reinterpret_cast<f32x4*>(mrg + 8 * i) = f32x4{acc[0][i], acc[1][i], acc[2][i], acc[3][i]}; \
        *reinterpret_cast<f32x4*>(mrg + 8 * i + 4) = f32x4{acc[4][i], acc[5][i], acc[6][i], acc[7][i]}; } \
      if (g == 0 && (c & 1) == 0) { lds_ml[P][wave][c >> 1][0] = m; lds_ml[P][wave][c >> 1][1] = l; } } } while (0)
#define COMPUTE
    if (wave < min(cnt, NACT)) { const unsigned pb_ = (unsigned)__builtin_amdgcn_readlane(offv, 0); issue_c(ta, kc + pb_, vc + pb_, lane, (__builtin_amdgcn_readlane(metav, 0) & 255) - 1); }
    lds_desc[wave + NW * lane] = uint2{(unsigned)offv, (unsigned)metav};
    if (tid == 0) lds_ctr = NACT;
    slotp = wg - wg_of(slotp);
    if (wave < npcs) *reinterpret_cast<u32x4*>(lds_q[wave] + (lane >> 5) * 576 + (lane & 31) * 16) = qst;
    if (first) { first = false;
      {
        const int b = wg >> 3, kvh = wg & 7;
        const int pb = __builtin_amdgcn_readlane(priv, b), st = 8 * (__builtin_amdgcn_readlane(incl, b) - pb) + kvh * pb;
        const int npc = pb > 0 ? wg_of(st + pb - 1) - wg_of(st) + 1 : 0;
        const int slot = wave;
        if ((slot < NPR && slot >= npc) || (slot >= NPR && slot < NPR + NSH && Ns == 0)) {
    #pragma unroll
          for (int h = 0; h < 2; ++h) {
            const size_t rs = (size_t)(b * H + 2 * kvh + h) * NSLOT + slot;
            if (lane == 0) *reinterpret_cast<f32x2*>(ws_ml + rs * 2) = f32x2{-INFINITY, 0.f};
          }
        }
      }
     }
    __syncthreads();
    STAMP(3);
    if (wave < min(cnt, NACT)) {
      int meta = __builtin_amdgcn_readlane(metav, 0), pc = meta >> 8;
      LOADQ(pc);
      for (;;) {
        kwrite(ta.k, ks, lc);
        int nx = 0;
        if (lane == 0) nx = __hip_atomic_fetch_add(&lds_ctr, 1, __ATOMIC_RELAXED, __HIP_MEMORY_SCOPE_WORKGROUP);
        nx = __builtin_amdgcn_readfirstlane(nx);
        const uint2 dn = lds_desc[min(nx, NW * 64 - 1)];
        const unsigned noff = __builtin_amdgcn_readfirstlane(dn.x);
        const int nmeta = nx < cnt ? (int)__builtin_amdgcn_readfirstlane(dn.y) : (NPC << 8);
#pragma unroll
        for (int j = 0; j < 8; ++j) tb.v[j] = ta.v[j];
        if (nx < cnt) issue_c(ta, kc + noff, vc + noff, lane, (nmeta & 255) - 1);
#pragma unroll
        for (int j = 0; j < 8; ++j) q[j] = *reinterpret_cast<const u32x4*>(qs + j * 64);
        core<false>(tb.v, q, meta & 255, ks, lc, m, l, acc);
        const int pn = nmeta >> 8;
        if (pn != pc) { FLUSH(pc); pc = pn; if (pc >= NPC) break; LOADQ(pc); }
        meta = nmeta;
      }
    }
#undef LOADQ
#undef FLUSH
#undef COMPUTE
    STAMP(4);
    __syncthreads();
    STAMP(5);
    {
      const int p = tid >> 7, h = (tid >> 6) & 1;
      if (p < npcs) {
        f32x2 mlw[NW]; f32x4 aw[NW];
#pragma unroll
        for (int w = 0; w < NW; ++w) {
          mlw[w] = *reinterpret_cast<const f32x2*>(lds_ml[p][w][h]);
          aw[w] = *reinterpret_cast<const f32x4*>(big + 65536 + (p * NW + w) * 2048 + h * 1024 + lane * 16);
        }
        float M = -INFINITY;
#pragma unroll
        for (int w = 0; w < NW; ++w) M = fmaxf(M, mlw[w][0]);
        float L = 0.f; f32x4 o = {0.f, 0.f, 0.f, 0.f};
#pragma unroll
        for (int w = 0; w < NW; ++w) {
          const bool ok = mlw[w][1] > 0.f;
          const float wt = ok ? __builtin_amdgcn_exp2f(mlw[w][0] - M) : 0.f;
          L = fmaf(wt, mlw[w][1], L);
#pragma unroll
          for (int e = 0; e < 4; ++e) o[e] = fmaf(ok ? aw[w][e] : 0.f, wt, o[e]);
        }
        const size_t rs = (size_t)(rowp + h) * NSLOT + slotp;
        stw(ws_acc + rs * D + lane * 4, o);
        if (lane == 0) *reinterpret_cast<f32x2*>(ws_ml + rs * 2) = f32x2{M, L};
      }
    }
    u = ue;
    if (u < u1) __syncthreads();
  }
  if (first) {
  {
    const int b = wg >> 3, kvh = wg & 7;
    const int pb = __builtin_amdgcn_readlane(priv, b), st = 8 * (__builtin_amdgcn_readlane(incl, b) - pb) + kvh * pb;
    const int npc = pb > 0 ? wg_of(st + pb - 1) - wg_of(st) + 1 : 0;
    const int slot = wave;
    if ((slot < NPR && slot >= npc) || (slot >= NPR && slot < NPR + NSH && Ns == 0)) {
#pragma unroll
      for (int h = 0; h < 2; ++h) {
        const size_t rs = (size_t)(b * H + 2 * kvh + h) * NSLOT + slot;
        if (lane == 0) *reinterpret_cast<f32x2*>(ws_ml + rs * 2) = f32x2{-INFINITY, 0.f};
      }
    }
  }
  }
  STAMP(6);
}

__global__ void __launch_bounds__(256)
attn_combine(const char* __restrict__ ws, const char* __restrict__ vc, const int* __restrict__ bt, const int* __restrict__ lens,
             __hip_bfloat16* __restrict__ out) {
  const int lane = threadIdx.x & 63, row = blockIdx.x * 4 + (threadIdx.x >> 6);
  const float* ml = reinterpret_cast<const float*>(ws + WS_ML) + (size_t)row * NSLOT * 2;
  const float* ac = reinterpret_cast<const float*>(ws + WS_ACC) + (size_t)row * NSLOT * D + lane * 4;
  constexpr int NS = NPR + NSH;
  f32x2 mlv[NS]; f32x4 a[NS];
#pragma unroll
  for (int s = 0; s < NS; ++s) { mlv[s] = *reinterpret_cast<const f32x2*>(ml + 2 * s); a[s] = ldw<f32x4>(ac + s * D); }
  const int b = row / H;
  const int len = min(max(lens[b], 0), LK);
  float M = -INFINITY;
#pragma unroll
  for (int s = 0; s < NS; ++s) M = fmaxf(M, mlv[s][0]);
  float L = 0.f; f32x4 o = {0.f, 0.f, 0.f, 0.f};
#pragma unroll
  for (int s = 0; s < NS; ++s) {
    const bool ok = mlv[s][1] > 0.f;
    const float wt = ok ? __builtin_amdgcn_exp2f(mlv[s][0] - M) : 0.f;
    L = fmaf(wt, mlv[s][1], L);
#pragma unroll
    for (int e = 0; e < 4; ++e) o[e] = fmaf(ok ? a[s][e] : 0.f, wt, o[e]);
  }
  const float MK = MASKED * LOG2E;
  if (len < LK && !(M > MK + 200.f)) {
    const float M2 = fmaxf(M, MK);
    const float w1 = (L > 0.f) ? __builtin_amdgcn_exp2f(M - M2) : 0.f, w2 = __builtin_amdgcn_exp2f(MK - M2);
    f32x4 sv = {0.f, 0.f, 0.f, 0.f};
    const int kvh = (row % H) >> 1;
    for (int t = len; t < LK; ++t) {
      const ushort4 r = *reinterpret_cast<const ushort4*>(vc + (size_t)bt[b * NB + (t >> 4)] * PAGE_BYTES + (size_t)(t & 15) * ROW_BYTES + kvh * 512 + lane * 8);
      sv[0] += __uint_as_float((unsigned)r.x << 16); sv[1] += __uint_as_float((unsigned)r.y << 16);
      sv[2] += __uint_as_float((unsigned)r.z << 16); sv[3] += __uint_as_float((unsigned)r.w << 16);
    }
    o = o * w1 + sv * w2; L = L * w1 + (float)(LK - len) * w2;
  }
  const float inv = 1.f / L;
  uint2 r = {cvtpk(o[0] * inv, o[1] * inv), cvtpk(o[2] * inv, o[3] * inv)};
  *reinterpret_cast<uint2*>(out + (size_t)row * D + lane * 4) = r;
}

extern "C" void kernel_launch(void* const* d_in, const int* in_sizes, int n_in, void* d_out, int out_size,
                              void* d_ws, size_t ws_size, hipStream_t stream) {
  attn_split<<<NWG, NW * 64, 0, stream>>>((const char*)d_in[0], (const char*)d_in[1], (const char*)d_in[2],
                                          (const int*)d_in[3], (const int*)d_in[4], (char*)d_ws);
  attn_combine<<<B * H / 4, 256, 0, stream>>>((const char*)d_ws, (const char*)d_in[2], (const int*)d_in[3], (const int*)d_in[4],
                                              (__hip_bfloat16*)d_out);
}
```

```cpp
#include <hip/hip_runtime.h>
#include <hip/hip_bf16.h>
#include <cstdint>

constexpr int B = 32, H = 16, HKV = 8, D = 256, BS = 16, NB = 128, LK = NB * BS;
constexpr int PAGE_BYTES = BS * HKV * D * 2;
constexpr int ROW_BYTES = HKV * D * 2;
constexpr int NW = 8, NWG = 256;
#define NSHV 3
constexpr int NSH = NSHV, NPR = 3, NSLOT = 8;
constexpr int NPC = 4;
#define CS_NUM 3
#define CS_DEN 2
#define CS_ADD 5
#define CODE_LINES 200
#define NACT 3
constexpr float LOG2E = 1.4426950408889634f;
constexpr float C2 = 0.0625f * LOG2E;
constexpr float MASKED = -998244352.0f;
constexpr size_t WS_ML = 0, WS_ACC = (size_t)B * H * NSLOT * 2 * 4;

using bf16x8 = __attribute__((ext_vector_type(8))) __bf16;
using u32x4 = __attribute__((ext_vector_type(4))) unsigned;
using f32x4 = __attribute__((ext_vector_type(4))) float;
using f32x2 = __attribute__((ext_vector_type(2))) float;
using bf16x2 = __attribute__((ext_vector_type(2))) __bf16;

template <int NT> __device__ __forceinline__ u32x4 ld16(const char* p) {
  if (NT) return __builtin_nontemporal_load(reinterpret_cast<const u32x4*>(p));
  return *reinterpret_cast<const u32x4*>(p);
}
#define NTS 0
template <typename T> __device__ __forceinline__ void stw(float* p, T v) {
  if (NTS & 1) __builtin_nontemporal_store(v, reinterpret_cast<T*>(p)); else *reinterpret_cast<T*>(p) = v;
}
template <typename T> __device__ __forceinline__ T ldw(const float* p) {
  if (NTS & 2) return __builtin_nontemporal_load(reinterpret_cast<const T*>(p)); else return *reinterpret_cast<const T*>(p);
}
__device__ __forceinline__ unsigned cvtpk(float lo, float hi) {
  return __builtin_bit_cast(unsigned, __builtin_convertvector(f32x2{lo, hi}, bf16x2));
}
__device__ __forceinline__ float xrow16_max(float x) {
  auto s = __builtin_amdgcn_permlane16_swap(__float_as_uint(x), __float_as_uint(x), false, false);
  x = fmaxf(__uint_as_float(s[0]), __uint_as_float(s[1]));
  auto t = __builtin_amdgcn_permlane32_swap(__float_as_uint(x), __float_as_uint(x), false, false);
  return fmaxf(__uint_as_float(t[0]), __uint_as_float(t[1]));
}
__device__ __forceinline__ float xrow16_sum(float x) {
  auto s = __builtin_amdgcn_permlane16_swap(__float_as_uint(x), __float_as_uint(x), false, false);
  x = __uint_as_float(s[0]) + __uint_as_float(s[1]);
  auto t = __builtin_amdgcn_permlane32_swap(__float_as_uint(x), __float_as_uint(x), false, false);
  return __uint_as_float(t[0]) + __uint_as_float(t[1]);
}

struct Tile { u32x4 k[8]; u32x4 v[8]; };

template <int CTRL> __device__ __forceinline__ int dpp_i(int x, int fill) { return __builtin_amdgcn_update_dpp(fill, x, CTRL, 0xf, 0xf, false); }
template <int CTRL, int RM> __device__ __forceinline__ int dpp_m(int x, int fill) { return __builtin_amdgcn_update_dpp(fill, x, CTRL, RM, 0xf, false); }
__device__ __forceinline__ int scan_add(int x) {
  x += dpp_i<0x111>(x, 0); x += dpp_i<0x112>(x, 0); x += dpp_i<0x114>(x, 0); x += dpp_i<0x118>(x, 0);
  x += dpp_m<0x142, 0xa>(x, 0); x += dpp_m<0x143, 0xc>(x, 0); return x;
}
__device__ __forceinline__ int scan_min(int x) {
  constexpr int F = 0x7fffffff;
  x = min(x, dpp_i<0x111>(x, F)); x = min(x, dpp_i<0x112>(x, F)); x = min(x, dpp_i<0x114>(x, F)); x = min(x, dpp_i<0x118>(x, F));
  x = min(x, dpp_m<0x142, 0xa>(x, F)); x = min(x, dpp_m<0x143, 0xc>(x, F)); return x;
}

template <int NT> __device__ __forceinline__ void issue(Tile& t, const char* kp, const char* vp) {
#pragma unroll
  for (int j = 0; j < 8; ++j) t.k[j] = ld16<NT>(kp + j * 2 * ROW_BYTES);
#pragma unroll
  for (int j = 0; j < 8; ++j) t.v[j] = ld16<NT>(vp + j * 2 * ROW_BYTES);
}

__device__ __forceinline__ void issue_c(Tile& t, const char* kp, const char* vp, int lane, int rl) {
  unsigned o[8];
#pragma unroll
  for (int j = 0; j < 8; ++j) o[j] = (unsigned)min(2 * j + (lane >> 5), rl) * ROW_BYTES + (lane & 31) * 16;
#pragma unroll
  for (int j = 0; j < 8; ++j) t.k[j] = ld16<1>(kp + o[j]);
#pragma unroll
  for (int j = 0; j < 8; ++j) t.v[j] = ld16<1>(vp + o[j]);
}

struct LaneC { int wr_off, rd_off, tok0; unsigned pmask; };

__device__ __forceinline__ void kwrite(const u32x4 (&k)[8], char* ks, const LaneC& lc) {
#pragma unroll
  for (int j = 0; j < 8; ++j) *reinterpret_cast<u32x4*>(ks + j * 1024 + (lc.wr_off ^ (j * 16))) = k[j];
}
__device__ __forceinline__ u32x4 vop(const u32x4 (&v)[8], int d) {
  const unsigned sel = (d & 1) ? 0x07060302u : 0x05040100u;
  return u32x4{__builtin_amdgcn_perm(v[1][d >> 1], v[0][d >> 1], sel), __builtin_amdgcn_perm(v[3][d >> 1], v[2][d >> 1], sel),
               __builtin_amdgcn_perm(v[5][d >> 1], v[4][d >> 1], sel), __builtin_amdgcn_perm(v[7][d >> 1], v[6][d >> 1], sel)};
}
template <bool PT>
__device__ __forceinline__ void core(const u32x4 (&v)[8], const u32x4 (&q)[8], int rem, const char* ks, const LaneC& lc,
                                     float& m, float& l, f32x4 (&acc)[8]) {
  f32x4 s0 = {0.f, 0.f, 0.f, 0.f}, s1 = {0.f, 0.f, 0.f, 0.f};
#pragma unroll
  for (int j = 0; j < 4; ++j) {
    const u32x4 ka = *reinterpret_cast<const u32x4*>(ks + (lc.rd_off ^ (2 * j * 64)));
    const u32x4 kb = *reinterpret_cast<const u32x4*>(ks + (lc.rd_off ^ ((2 * j + 1) * 64)));
    s0 = __builtin_amdgcn_mfma_f32_16x16x32_bf16(__builtin_bit_cast(bf16x8, ka), __builtin_bit_cast(bf16x8, q[2 * j]), s0, 0, 0, 0);
    s1 = __builtin_amdgcn_mfma_f32_16x16x32_bf16(__builtin_bit_cast(bf16x8, kb), __builtin_bit_cast(bf16x8, q[2 * j + 1]), s1, 0, 0, 0);
  }
  float x[4];
#pragma unroll
  for (int i = 0; i < 4; ++i) {
    x[i] = (s0[i] + s1[i]) * C2;
    if (rem < BS) x[i] = (lc.tok0 + 2 * i < rem) ? x[i] : -INFINITY;
  }
  float mx = fmaxf(fmaxf(x[0], x[1]), fmaxf(x[2], x[3]));
  mx = xrow16_max(mx);
  const float mn = fmaxf(m, mx);
  const float msub = (mn == -INFINITY) ? 0.f : mn;
  const float alpha = __builtin_amdgcn_exp2f(m - msub);
  float p[4];
#pragma unroll
  for (int i = 0; i < 4; ++i) p[i] = __builtin_amdgcn_exp2f(x[i] - msub);
  l = fmaf(l, alpha, (p[0] + p[1]) + (p[2] + p[3]));
  if (__builtin_amdgcn_ballot_w64(mn != m) != 0) {
#pragma unroll
    for (int u = 0; u < 8; ++u) acc[u] *= alpha;
  }
  m = mn;
  const unsigned p01 = cvtpk(p[0], p[1]), p23 = cvtpk(p[2], p[3]);
  auto sa = __builtin_amdgcn_permlane16_swap(p01, p01, false, false);
  auto sb = __builtin_amdgcn_permlane16_swap(p23, p23, false, false);
  const u32x4 pb = {sa[0] & lc.pmask, sb[0] & lc.pmask, sa[1] & lc.pmask, sb[1] & lc.pmask};
#pragma unroll
  for (int d = 0; d < 8; ++d) {
    const u32x4 a = PT ? v[d] : vop(v, d);
    acc[d] = __builtin_amdgcn_mfma_f32_16x16x32_bf16(__builtin_bit_cast(bf16x8, a), __builtin_bit_cast(bf16x8, pb), acc[d], 0, 0, 0);
  }
}

__global__ void __launch_bounds__(NW * 64, 2)
attn_split(const char* __restrict__ qg, const char* __restrict__ kc, const char* __restrict__ vc,
           const int* __restrict__ bt, const int* __restrict__ lens, char* __restrict__ ws) {
  __shared__ __attribute__((aligned(16))) char big[131072];
  __shared__ __attribute__((aligned(16))) int lds_bt[B * NB];
  __shared__ __attribute__((aligned(16))) char lds_z[512];
  __shared__ __attribute__((aligned(16))) int lds_min[NW];
  __shared__ __attribute__((aligned(8))) float lds_ml[NPC][NW][2][2];
  const int wg = blockIdx.x, tid = threadIdx.x, lane = tid & 63, wave = __builtin_amdgcn_readfirstlane(tid >> 6);
  const int g = lane >> 4, c = lane & 15;
  float* ws_ml = reinterpret_cast<float*>(ws + WS_ML);
  float* ws_acc = reinterpret_cast<float*>(ws + WS_ACC);
  int len = lens[lane & 31];
  unsigned pf = 0;
  if (tid < CODE_LINES) {
    unsigned long long pc; asm volatile("s_getpc_b64 %0" : "=s"(pc));
    pf = *reinterpret_cast<const unsigned*>((pc & ~255ull) + tid * 64);
  }
  {
    const int col = (tid & 15) * 8;
    const u32x4 a0 = *reinterpret_cast<const u32x4*>(bt + tid * 8), a1 = *reinterpret_cast<const u32x4*>(bt + tid * 8 + 4);
    const u32x4 r0 = *reinterpret_cast<const u32x4*>(bt + col), r1 = *reinterpret_cast<const u32x4*>(bt + col + 4);
    *reinterpret_cast<u32x4*>(lds_bt + tid * 8) = a0; *reinterpret_cast<u32x4*>(lds_bt + tid * 8 + 4) = a1;
    int fm = NB;
#pragma unroll
    for (int e = 3; e >= 0; --e) if (a1[e] != r1[e]) fm = col + 4 + e;
#pragma unroll
    for (int e = 3; e >= 0; --e) if (a0[e] != r0[e]) fm = col + e;
    fm = scan_min(fm);
    if (lane == 63) lds_min[wave] = fm;
    if (tid < 32) *reinterpret_cast<u32x4*>(lds_z + tid * 16) = u32x4{0u, 0u, 0u, 0u};
  }
  asm volatile("" :: "v"(pf));
  len = min(max(len, 0), LK);
  const int npg = (len + BS - 1) / BS;
  const int minlen = __builtin_amdgcn_readlane(scan_min(len), 31), mxn = -__builtin_amdgcn_readlane(scan_min(-npg), 31);
  const int incn = scan_add(npg);
  __syncthreads();
  int S = minlen / BS;
  {
    const int4 m0 = *reinterpret_cast<const int4*>(lds_min), m1 = *reinterpret_cast<const int4*>(lds_min + 4);
    S = min(min(min(S, m0.x), min(m0.y, m0.z)), min(min(m0.w, m1.x), min(min(m1.y, m1.z), m1.w)));
  }
  S = __builtin_amdgcn_readfirstlane(S);
  const int priv = npg - S;
  const int incl = incn - S * (lane + 1);
  const int Tp = 8 * __builtin_amdgcn_readlane(incl, 31), mxp = mxn - S;
  const int Ns = S > 0 ? 8 * NSH : 0, per = (S + NSH - 1) / NSH, cs = S > 0 ? per * CS_NUM / CS_DEN + CS_ADD : 0;
  const int umin = max((mxp + NPR - 2) / (NPR - 1), 1);
  int U = max((Tp + Ns * cs + NWG - 1) / NWG, umin), Us = U - cs;
  if (Us < umin) { Us = 0; U = max(Ns ? (Tp + (NWG - 8 * NSH) - 1) / (NWG - 8 * NSH) : (Tp + NWG - 1) / NWG, umin); }
  const int usplit = Ns * Us;
  auto wg_of = [&](int u) { return u < usplit ? u / Us : Ns + (u - usplit) / U; };
  LaneC lc;
  lc.wr_off = (lane >> 5) * 512 + (((lane & 31) ^ (8 * (lane >> 5))) * 16);
  {
    const int tk = 8 * ((c >> 2) & 1) + 2 * (c & 3) + (c >> 3);
    lc.rd_off = tk * 512 + ((g ^ c) * 16);
  }
  lc.tok0 = 8 * (g & 1) + (g >> 1);
  lc.pmask = ((g & 1) == (c & 1)) ? 0xffffffffu : 0u;
  const int loff = (lane >> 5) * ROW_BYTES + (lane & 31) * 16;
  const char* kl = kc + loff;
  const char* vl = vc + loff;
  u32x4 q[8];
  float m, l;
  f32x4 acc[8];
  Tile ta, tb;
  if (wg < Ns) {
    const int kvh = wg & 7, sp = wg >> 3;
    const int p0 = min(sp * per, S), n = min(p0 + per, S) - p0;
    const int qr = c >> 1, row = (4 * wave + (qr >> 1)) * H + 2 * kvh + (qr & 1);
    if (n > 0) { const size_t pb_ = (size_t)lds_bt[p0 + min(wave, n - 1)] * PAGE_BYTES + kvh * 512; issue<1>(ta, kl + pb_, vl + pb_); }
#pragma unroll
    for (int j = 0; j < 8; ++j) q[j] = *reinterpret_cast<const u32x4*>(qg + (size_t)row * (D * 2) + (4 * j + g) * 16);
    m = -INFINITY; l = 0.f;
#pragma unroll
    for (int u = 0; u < 8; ++u) acc[u] = f32x4{0.f, 0.f, 0.f, 0.f};
    for (int r = 0; r < n; r += NW) {
      char* st = big + wave * 16384;
      kwrite(ta.k, st, lc);
#pragma unroll
      for (int j = 0; j < 8; ++j) *reinterpret_cast<u32x4*>(st + 8192 + j * 1024 + lane * 16) = vop(ta.v, j);
      { const size_t pb_ = (size_t)lds_bt[p0 + min(r + NW + wave, n - 1)] * PAGE_BYTES + kvh * 512; if (r + NW < n) issue<1>(ta, kl + pb_, vl + pb_); }
      __syncthreads();
      const int cnt = min(NW, n - r);
      for (int pg = 0; pg < cnt; ++pg) {
        const char* sp_ = big + pg * 16384;
#pragma unroll
        for (int j = 0; j < 8; ++j) tb.v[j] = *reinterpret_cast<const u32x4*>(sp_ + 8192 + j * 1024 + lane * 16);
        core<true>(tb.v, q, BS, sp_, lc, m, l, acc);
      }
      __syncthreads();
    }
    l = xrow16_sum(l);
    const size_t rs = (size_t)row * NSLOT + NPR + sp;
    {
      float* tr = reinterpret_cast<float*>(big + wave * 16384);
#pragma unroll
      for (int i = 0; i < 4; ++i) {
        float* dst = tr + qr * (D + 4) + 128 * (c & 1) + 32 * g + 8 * i;
        *reinterpret_cast<f32x4*>(dst) = f32x4{acc[0][i], acc[1][i], acc[2][i], acc[3][i]};
        *reinterpret_cast<f32x4*>(dst + 4) = f32x4{acc[4][i], acc[5][i], acc[6][i], acc[7][i]};
      }
      __builtin_amdgcn_wave_barrier();
#pragma unroll
      for (int r = 0; r < 8; ++r) {
        const size_t rr = (size_t)((4 * wave + (r >> 1)) * H + 2 * kvh + (r & 1)) * NSLOT + NPR + sp;
        stw(ws_acc + rr * D + lane * 4, *reinterpret_cast<const f32x4*>(tr + r * (D + 4) + lane * 4));
      }
      __builtin_amdgcn_wave_barrier();
    }
    if (g == 0 && (c & 1) == 0) *reinterpret_cast<f32x2*>(ws_ml + rs * 2) = f32x2{m, l};
    __syncthreads();
  }
  char* ks = big + wave * 8192;
  char* qw = big + 32768 + wave * (NPC * 1152);
  const char* qb = c < 4 ? qw + (c >> 1) * 576 + g * 16 : lds_z + g * 16;
  const int qst = c < 4 ? 1152 : 0;
  const char* qs = qb;
  int u = wg < Ns ? wg * Us : usplit + (wg - Ns) * U;
  const int u1 = min(u + (wg < Ns ? Us : U), Tp);
  bool first = true;
  while (u < u1) {
    const int uu = u + wave + NACT * lane;
    int offv = 0, remv = BS, pcv = NPC, ue = u, rowp = 0, slotp = 0, npcs = 0, qrow = 0;
#pragma unroll
    for (int p = 0; p < NPC; ++p) {
      if (ue < u1) {
        const int b = __builtin_ctzll(__builtin_amdgcn_ballot_w64(lane < 32 && 8 * incl > ue));
        const int pb = __builtin_amdgcn_readlane(priv, b), st = 8 * (__builtin_amdgcn_readlane(incl, b) - pb);
        const int lb = __builtin_amdgcn_readlane(len, b);
        const int off = ue - st, kvh = __builtin_popcountll(__builtin_amdgcn_ballot_w64(lane < 7 && (lane + 1) * pb <= off)), pg0 = off - kvh * pb, np = min(pb - pg0, u1 - ue);
        if (uu >= ue && uu < ue + np) {
          const int lp = S + pg0 + (uu - ue);
          offv = lds_bt[b * NB + lp] * PAGE_BYTES + kvh * 512; remv = lb - lp * BS; pcv = p;
        }
        if (lane == p) qrow = b * H + 2 * kvh;
        if ((tid >> 7) == p) { rowp = b * H + 2 * kvh; slotp = st + kvh * pb; }
        ue += np; npcs = p + 1;
      }
    }
    const int cnt = ue - u;
    const int metav = min(remv, BS) | (pcv << 8);
    if (lane < NPC * 2) { lds_ml[lane >> 1][wave][lane & 1][0] = -INFINITY; lds_ml[lane >> 1][wave][lane & 1][1] = 0.f; }
#define LOADQ(P) do { qs = qb + (P) * qst; m = -INFINITY; l = 0.f; _Pragma("unroll") for (int v = 0; v < 8; ++v) acc[v] = f32x4{0.f, 0.f, 0.f, 0.f}; } while (0)
#define FLUSH(P) do { l = xrow16_sum(l); if (c < 4) { \
      float* mrg = reinterpret_cast<float*>(big + 65536 + ((P) * NW + wave) * 2048) + (c >> 1) * D + 128 * (c & 1) + 32 * g; \
      _Pragma("unroll") for (int i = 0; i < 4; ++i) { \
        *reinterpret_cast<f32x4*>(mrg + 8 * i) = f32x4{acc[0][i], acc[1][i], acc[2][i], acc[3][i]}; \
        *reinterpret_cast<f32x4*>(mrg + 8 * i + 4) = f32x4{acc[4][i], acc[5][i], acc[6][i], acc[7][i]}; } \
      if (g == 0 && (c & 1) == 0) { lds_ml[P][wave][c >> 1][0] = m; lds_ml[P][wave][c >> 1][1] = l; } } } while (0)
    if (wave < min(cnt, NACT)) {
#pragma unroll
      for (int p = 0; p < NPC; ++p) q[p] = *reinterpret_cast<const u32x4*>(qg + (size_t)__builtin_amdgcn_readlane(qrow, p) * (D * 2) + lane * 16);
      const unsigned pb_ = (unsigned)__builtin_amdgcn_readlane(offv, 0);
      issue_c(ta, kc + pb_, vc + pb_, lane, (__builtin_amdgcn_readlane(metav, 0) & 255) - 1);
#pragma unroll
      for (int p = 0; p < NPC; ++p) *reinterpret_cast<u32x4*>(qw + p * 1152 + (lane >> 5) * 576 + (lane & 31) * 16) = q[p];
    }
    slotp = wg - wg_of(slotp);
    if (first) { first = false;
      {
        const int b = wg >> 3, kvh = wg & 7;
        const int pb = __builtin_amdgcn_readlane(priv, b), st = 8 * (__builtin_amdgcn_readlane(incl, b) - pb) + kvh * pb;
        const int npc = pb > 0 ? wg_of(st + pb - 1) - wg_of(st) + 1 : 0;
        const int slot = wave;
        if ((slot < NPR && slot >= npc) || (slot >= NPR && slot < NPR + NSH && Ns == 0)) {
    #pragma unroll
          for (int h = 0; h < 2; ++h) {
            const size_t rs = (size_t)(b * H + 2 * kvh + h) * NSLOT + slot;
            if (lane == 0) *reinterpret_cast<f32x2*>(ws_ml + rs * 2) = f32x2{-INFINITY, 0.f};
          }
        }
      }
     }
    if (wave < min(cnt, NACT)) {
      int meta = __builtin_amdgcn_readlane(metav, 0), pc = meta >> 8;
      LOADQ(pc);
      for (int i = 1;; ++i) {
        kwrite(ta.k, ks, lc);
        const unsigned noff = (unsigned)__builtin_amdgcn_readlane(offv, i);
        const int nmeta = __builtin_amdgcn_readlane(metav, i);
#pragma unroll
        for (int j = 0; j < 8; ++j) tb.v[j] = ta.v[j];
        if (nmeta < (NPC << 8)) issue_c(ta, kc + noff, vc + noff, lane, (nmeta & 255) - 1);
#pragma unroll
        for (int j = 0; j < 8; ++j) q[j] = *reinterpret_cast<const u32x4*>(qs + j * 64);
        core<false>(tb.v, q, meta & 255, ks, lc, m, l, acc);
        const int pn = nmeta >> 8;
        if (pn != pc) { FLUSH(pc); pc = pn; if (pc >= NPC) break; LOADQ(pc); }
        meta = nmeta;
      }
    }
#undef LOADQ
#undef FLUSH
    __syncthreads();
    {
      const int p = tid >> 7, h = (tid >> 6) & 1;
      if (p < npcs) {
        f32x2 mlw[NW]; f32x4 aw[NW];
#pragma unroll
        for (int w = 0; w < NW; ++w) {
          mlw[w] = *reinterpret_cast<const f32x2*>(lds_ml[p][w][h]);
          aw[w] = *reinterpret_cast<const f32x4*>(big + 65536 + (p * NW + w) * 2048 + h * 1024 + lane * 16);
        }
        float M = -INFINITY;
#pragma unroll
        for (int w = 0; w < NW; ++w) M = fmaxf(M, mlw[w][0]);
        float L = 0.f; f32x4 o = {0.f, 0.f, 0.f, 0.f};
#pragma unroll
        for (int w = 0; w < NW; ++w) {
          const bool ok = mlw[w][1] > 0.f;
          const float wt = ok ? __builtin_amdgcn_exp2f(mlw[w][0] - M) : 0.f;
          L = fmaf(wt, mlw[w][1], L);
#pragma unroll
          for (int e = 0; e < 4; ++e) o[e] = fmaf(ok ? aw[w][e] : 0.f, wt, o[e]);
        }
        const size_t rs = (size_t)(rowp + h) * NSLOT + slotp;
        stw(ws_acc + rs * D + lane * 4, o);
        if (lane == 0) *reinterpret_cast<f32x2*>(ws_ml + rs * 2) = f32x2{M, L};
      }
    }
    u = ue;
    if (u < u1) __syncthreads();
  }
  if (first) {
  {
    const int b = wg >> 3, kvh = wg & 7;
    const int pb = __builtin_amdgcn_readlane(priv, b), st = 8 * (__builtin_amdgcn_readlane(incl, b) - pb) + kvh * pb;
    const int npc = pb > 0 ? wg_of(st + pb - 1) - wg_of(st) + 1 : 0;
    const int slot = wave;
    if ((slot < NPR && slot >= npc) || (slot >= NPR && slot < NPR + NSH && Ns == 0)) {
#pragma unroll
      for (int h = 0; h < 2; ++h) {
        const size_t rs = (size_t)(b * H + 2 * kvh + h) * NSLOT + slot;
        if (lane == 0) *reinterpret_cast<f32x2*>(ws_ml + rs * 2) = f32x2{-INFINITY, 0.f};
      }
    }
  }
  }
}

__global__ void __launch_bounds__(256)
attn_combine(const char* __restrict__ ws, const char* __restrict__ vc, const int* __restrict__ bt, const int* __restrict__ lens,
             __hip_bfloat16* __restrict__ out) {
  const int lane = threadIdx.x & 63, row = blockIdx.x * 4 + (threadIdx.x >> 6);
  const float* ml = reinterpret_cast<const float*>(ws + WS_ML) + (size_t)row * NSLOT * 2;
  const float* ac = reinterpret_cast<const float*>(ws + WS_ACC) + (size_t)row * NSLOT * D + lane * 4;
  constexpr int NS = NPR + NSH;
  f32x2 mlv[NS]; f32x4 a[NS];
#pragma unroll
  for (int s = 0; s < NS; ++s) { mlv[s] = *reinterpret_cast<const f32x2*>(ml + 2 * s); a[s] = ldw<f32x4>(ac + s * D); }
  const int b = row / H;
  const int len = min(max(lens[b], 0), LK);
  float M = -INFINITY;
#pragma unroll
  for (int s = 0; s < NS; ++s) M = fmaxf(M, mlv[s][0]);
  float L = 0.f; f32x4 o = {0.f, 0.f, 0.f, 0.f};
#pragma unroll
  for (int s = 0; s < NS; ++s) {
    const bool ok = mlv[s][1] > 0.f;
    const float wt = ok ? __builtin_amdgcn_exp2f(mlv[s][0] - M) : 0.f;
    L = fmaf(wt, mlv[s][1], L);
#pragma unroll
    for (int e = 0; e < 4; ++e) o[e] = fmaf(ok ? a[s][e] : 0.f, wt, o[e]);
  }
  const float MK = MASKED * LOG2E;
  if (len < LK && !(M > MK + 200.f)) {
    const float M2 = fmaxf(M, MK);
    const float w1 = (L > 0.f) ? __builtin_amdgcn_exp2f(M - M2) : 0.f, w2 = __builtin_amdgcn_exp2f(MK - M2);
    f32x4 sv = {0.f, 0.f, 0.f, 0.f};
    const int kvh = (row % H) >> 1;
    for (int t = len; t < LK; ++t) {
      const ushort4 r = *reinterpret_cast<const ushort4*>(vc + (size_t)bt[b * NB + (t >> 4)] * PAGE_BYTES + (size_t)(t & 15) * ROW_BYTES + kvh * 512 + lane * 8);
      sv[0] += __uint_as_float((unsigned)r.x << 16); sv[1] += __uint_as_float((unsigned)r.y << 16);
      sv[2] += __uint_as_float((unsigned)r.z << 16); sv[3] += __uint_as_float((unsigned)r.w << 16);
    }
    o = o * w1 + sv * w2; L = L * w1 + (float)(LK - len) * w2;
  }
  const float inv = 1.f / L;
  uint2 r = {cvtpk(o[0] * inv, o[1] * inv), cvtpk(o[2] * inv, o[3] * inv)};
  *reinterpret_cast<uint2*>(out + (size_t)row * D + lane * 4) = r;
}

extern "C" void kernel_launch(void* const* d_in, const int* in_sizes, int n_in, void* d_out, int out_size,
                              void* d_ws, size_t ws_size, hipStream_t stream) {
  attn_split<<<NWG, NW * 64, 0, stream>>>((const char*)d_in[0], (const char*)d_in[1], (const char*)d_in[2],
                                          (const int*)d_in[3], (const int*)d_in[4], (char*)d_ws);
  attn_combine<<<B * H / 4, 256, 0, stream>>>((const char*)d_ws, (const char*)d_in[2], (const int*)d_in[3], (const int*)d_in[4],
                                              (__hip_bfloat16*)d_out);
}
```

```cpp
#include <hip/hip_runtime.h>
#include <hip/hip_bf16.h>
#include <cstdint>

constexpr int B = 32, H = 16, HKV = 8, D = 256, BS = 16, NB = 128, LK = NB * BS;
constexpr int PAGE_BYTES = BS * HKV * D * 2;
constexpr int ROW_BYTES = HKV * D * 2;
constexpr int NW = 8, NWG = 256;
#define NSHV 3
constexpr int NSH = NSHV, NPR = 3, NSLOT = 8;
constexpr int NPC = 4;
#define CS_NUM 3
#define CS_DEN 2
#define CS_ADD 5
#define CODE_LINES 200
#define NACT 5
constexpr float LOG2E = 1.4426950408889634f;
constexpr float C2 = 0.0625f * LOG2E;
constexpr float MASKED = -998244352.0f;
constexpr size_t WS_ML = 0, WS_ACC = (size_t)B * H * NSLOT * 2 * 4;

using bf16x8 = __attribute__((ext_vector_type(8))) __bf16;
using u32x4 = __attribute__((ext_vector_type(4))) unsigned;
using f32x4 = __attribute__((ext_vector_type(4))) float;
using f32x2 = __attribute__((ext_vector_type(2))) float;
using bf16x2 = __attribute__((ext_vector_type(2))) __bf16;

template <int NT> __device__ __forceinline__ u32x4 ld16(const char* p) {
  if (NT) return __builtin_nontemporal_load(reinterpret_cast<const u32x4*>(p));
  return *reinterpret_cast<const u32x4*>(p);
}
#define NTS 0
template <typename T> __device__ __forceinline__ void stw(float* p, T v) {
  if (NTS & 1) __builtin_nontemporal_store(v, reinterpret_cast<T*>(p)); else *reinterpret_cast<T*>(p) = v;
}
template <typename T> __device__ __forceinline__ T ldw(const float* p) {
  if (NTS & 2) return __builtin_nontemporal_load(reinterpret_cast<const T*>(p)); else return *reinterpret_cast<const T*>(p);
}
__device__ __forceinline__ unsigned cvtpk(float lo, float hi) {
  return __builtin_bit_cast(unsigned, __builtin_convertvector(f32x2{lo, hi}, bf16x2));
}
__device__ __forceinline__ float xrow16_max(float x) {
  auto s = __builtin_amdgcn_permlane16_swap(__float_as_uint(x), __float_as_uint(x), false, false);
  x = fmaxf(__uint_as_float(s[0]), __uint_as_float(s[1]));
  auto t = __builtin_amdgcn_permlane32_swap(__float_as_uint(x), __float_as_uint(x), false, false);
  return fmaxf(__uint_as_float(t[0]), __uint_as_float(t[1]));
}
__device__ __forceinline__ float xrow16_sum(float x) {
  auto s = __builtin_amdgcn_permlane16_swap(__float_as_uint(x), __float_as_uint(x), false, false);
  x = __uint_as_float(s[0]) + __uint_as_float(s[1]);
  auto t = __builtin_amdgcn_permlane32_swap(__float_as_uint(x), __float_as_uint(x), false, false);
  return __uint_as_float(t[0]) + __uint_as_float(t[1]);
}

struct Tile { u32x4 k[8]; u32x4 v[8]; };

template <int CTRL> __device__ __forceinline__ int dpp_i(int x, int fill) { return __builtin_amdgcn_update_dpp(fill, x, CTRL, 0xf, 0xf, false); }
template <int CTRL, int RM> __device__ __forceinline__ int dpp_m(int x, int fill) { return __builtin_amdgcn_update_dpp(fill, x, CTRL, RM, 0xf, false); }
__device__ __forceinline__ int scan_add(int x) {
  x += dpp_i<0x111>(x, 0); x += dpp_i<0x112>(x, 0); x += dpp_i<0x114>(x, 0); x += dpp_i<0x118>(x, 0);
  x += dpp_m<0x142, 0xa>(x, 0); x += dpp_m<0x143, 0xc>(x, 0); return x;
}
__device__ __forceinline__ int scan_min(int x) {
  constexpr int F = 0x7fffffff;
  x = min(x, dpp_i<0x111>(x, F)); x = min(x, dpp_i<0x112>(x, F)); x = min(x, dpp_i<0x114>(x, F)); x = min(x, dpp_i<0x118>(x, F));
  x = min(x, dpp_m<0x142, 0xa>(x, F)); x = min(x, dpp_m<0x143, 0xc>(x, F)); return x;
}

template <int NT> __device__ __forceinline__ void issue(Tile& t, const char* kp, const char* vp) {
#pragma unroll
  for (int j = 0; j < 8; ++j) t.k[j] = ld16<NT>(kp + j * 2 * ROW_BYTES);
#pragma unroll
  for (int j = 0; j < 8; ++j) t.v[j] = ld16<NT>(vp + j * 2 * ROW_BYTES);
}

__device__ __forceinline__ void issue_c(Tile& t, const char* kp, const char* vp, int lane, int rl) {
  unsigned o[8];
#pragma unroll
  for (int j = 0; j < 8; ++j) o[j] = (unsigned)min(2 * j + (lane >> 5), rl) * ROW_BYTES + (lane & 31) * 16;
#pragma unroll
  for (int j = 0; j < 8; ++j) t.k[j] = ld16<1>(kp + o[j]);
#pragma unroll
  for (int j = 0; j < 8; ++j) t.v[j] = ld16<1>(vp + o[j]);
}

struct LaneC { int wr_off, rd_off, tok0; unsigned pmask; };

__device__ __forceinline__ void kwrite(const u32x4 (&k)[8], char* ks, const LaneC& lc) {
#pragma unroll
  for (int j = 0; j < 8; ++j) *reinterpret_cast<u32x4*>(ks + j * 1024 + (lc.wr_off ^ (j * 16))) = k[j];
}
__device__ __forceinline__ u32x4 vop(const u32x4 (&v)[8], int d) {
  const unsigned sel = (d & 1) ? 0x07060302u : 0x05040100u;
  return u32x4{__builtin_amdgcn_perm(v[1][d >> 1], v[0][d >> 1], sel), __builtin_amdgcn_perm(v[3][d >> 1], v[2][d >> 1], sel),
               __builtin_amdgcn_perm(v[5][d >> 1], v[4][d >> 1], sel), __builtin_amdgcn_perm(v[7][d >> 1], v[6][d >> 1], sel)};
}
template <bool PT>
__device__ __forceinline__ void core(const u32x4 (&v)[8], const u32x4 (&q)[8], int rem, const char* ks, const LaneC& lc,
                                     float& m, float& l, f32x4 (&acc)[8]) {
  f32x4 s0 = {0.f, 0.f, 0.f, 0.f}, s1 = {0.f, 0.f, 0.f, 0.f};
#pragma unroll
  for (int j = 0; j < 4; ++j) {
    const u32x4 ka = *reinterpret_cast<const u32x4*>(ks + (lc.rd_off ^ (2 * j * 64)));
    const u32x4 kb = *reinterpret_cast<const u32x4*>(ks + (lc.rd_off ^ ((2 * j + 1) * 64)));
    s0 = __builtin_amdgcn_mfma_f32_16x16x32_bf16(__builtin_bit_cast(bf16x8, ka), __builtin_bit_cast(bf16x8, q[2 * j]), s0, 0, 0, 0);
    s1 = __builtin_amdgcn_mfma_f32_16x16x32_bf16(__builtin_bit_cast(bf16x8, kb), __builtin_bit_cast(bf16x8, q[2 * j + 1]), s1, 0, 0, 0);
  }
  float x[4];
#pragma unroll
  for (int i = 0; i < 4; ++i) {
    x[i] = (s0[i] + s1[i]) * C2;
    if (rem < BS) x[i] = (lc.tok0 + 2 * i < rem) ? x[i] : -INFINITY;
  }
  float mx = fmaxf(fmaxf(x[0], x[1]), fmaxf(x[2], x[3]));
  mx = xrow16_max(mx);
  const float mn = fmaxf(m, mx);
  const float msub = (mn == -INFINITY) ? 0.f : mn;
  const float alpha = __builtin_amdgcn_exp2f(m - msub);
  float p[4];
#pragma unroll
  for (int i = 0; i < 4; ++i) p[i] = __builtin_amdgcn_exp2f(x[i] - msub);
  l = fmaf(l, alpha, (p[0] + p[1]) + (p[2] + p[3]));
  if (__builtin_amdgcn_ballot_w64(mn != m) != 0) {
#pragma unroll
    for (int u = 0; u < 8; ++u) acc[u] *= alpha;
  }
  m = mn;
  const unsigned p01 = cvtpk(p[0], p[1]), p23 = cvtpk(p[2], p[3]);
  auto sa = __builtin_amdgcn_permlane16_swap(p01, p01, false, false);
  auto sb = __builtin_amdgcn_permlane16_swap(p23, p23, false, false);
  const u32x4 pb = {sa[0] & lc.pmask, sb[0] & lc.pmask, sa[1] & lc.pmask, sb[1] & lc.pmask};
#pragma unroll
  for (int d = 0; d < 8; ++d) {
    const u32x4 a = PT ? v[d] : vop(v, d);
    acc[d] = __builtin_amdgcn_mfma_f32_16x16x32_bf16(__builtin_bit_cast(bf16x8, a), __builtin_bit_cast(bf16x8, pb), acc[d], 0, 0, 0);
  }
}

__global__ void __launch_bounds__(NW * 64, 2)
attn_split(const char* __restrict__ qg, const char* __restrict__ kc, const char* __restrict__ vc,
           const int* __restrict__ bt, const int* __restrict__ lens, char* __restrict__ ws) {
  __shared__ __attribute__((aligned(16))) char big[131072];
  __shared__ __attribute__((aligned(16))) int lds_bt[B * NB];
  __shared__ __attribute__((aligned(16))) char lds_z[512];
  __shared__ __attribute__((aligned(16))) int lds_min[NW];
  __shared__ __attribute__((aligned(8))) float lds_ml[NPC][NW][2][2];
  const int wg = blockIdx.x, tid = threadIdx.x, lane = tid & 63, wave = __builtin_amdgcn_readfirstlane(tid >> 6);
  const int g = lane >> 4, c = lane & 15;
  float* ws_ml = reinterpret_cast<float*>(ws + WS_ML);
  float* ws_acc = reinterpret_cast<float*>(ws + WS_ACC);
  int len = lens[lane & 31];
  unsigned pf;
  unsigned long long pc; asm volatile("s_getpc_b64 %0" : "=s"(pc));
  {
    const int col = (tid & 15) * 8;
    const u32x4 a0 = *reinterpret_cast<const u32x4*>(bt + tid * 8), a1 = *reinterpret_cast<const u32x4*>(bt + tid * 8 + 4);
    const u32x4 r0 = *reinterpret_cast<const u32x4*>(bt + col), r1 = *reinterpret_cast<const u32x4*>(bt + col + 4);
    __builtin_amdgcn_sched_barrier(0);
    pf = *(const __attribute__((address_space(1))) unsigned*)((pc & ~255ull) + min(511 - tid, CODE_LINES - 1) * 64);
    __builtin_amdgcn_sched_barrier(0);
    *reinterpret_cast<u32x4*>(lds_bt + tid * 8) = a0; *reinterpret_cast<u32x4*>(lds_bt + tid * 8 + 4) = a1;
    int fm = NB;
#pragma unroll
    for (int e = 3; e >= 0; --e) if (a1[e] != r1[e]) fm = col + 4 + e;
#pragma unroll
    for (int e = 3; e >= 0; --e) if (a0[e] != r0[e]) fm = col + e;
    fm = scan_min(fm);
    if (lane == 63) lds_min[wave] = fm;
    if (tid < 32) *reinterpret_cast<u32x4*>(lds_z + tid * 16) = u32x4{0u, 0u, 0u, 0u};
  }
  len = min(max(len, 0), LK);
  const int npg = (len + BS - 1) / BS;
  const int minlen = __builtin_amdgcn_readlane(scan_min(len), 31), mxn = -__builtin_amdgcn_readlane(scan_min(-npg), 31);
  const int incn = scan_add(npg);
  __syncthreads();
  int S = minlen / BS;
  {
    const int4 m0 = *reinterpret_cast<const int4*>(lds_min), m1 = *reinterpret_cast<const int4*>(lds_min + 4);
    S = min(min(min(S, m0.x), min(m0.y, m0.z)), min(min(m0.w, m1.x), min(min(m1.y, m1.z), m1.w)));
  }
  S = __builtin_amdgcn_readfirstlane(S);
  const int priv = npg - S;
  const int incl = incn - S * (lane + 1);
  const int Tp = 8 * __builtin_amdgcn_readlane(incl, 31), mxp = mxn - S;
  const int Ns = S > 0 ? 8 * NSH : 0, per = (S + NSH - 1) / NSH, cs = S > 0 ? per * CS_NUM / CS_DEN + CS_ADD : 0;
  const int umin = max((mxp + NPR - 2) / (NPR - 1), 1);
  int U = max((Tp + Ns * cs + NWG - 1) / NWG, umin), Us = U - cs;
  if (Us < umin) { Us = 0; U = max(Ns ? (Tp + (NWG - 8 * NSH) - 1) / (NWG - 8 * NSH) : (Tp + NWG - 1) / NWG, umin); }
  const int usplit = Ns * Us;
  auto wg_of = [&](int u) { return u < usplit ? u / Us : Ns + (u - usplit) / U; };
  LaneC lc;
  lc.wr_off = (lane >> 5) * 512 + (((lane & 31) ^ (8 * (lane >> 5))) * 16);
  {
    const int tk = 8 * ((c >> 2) & 1) + 2 * (c & 3) + (c >> 3);
    lc.rd_off = tk * 512 + ((g ^ c) * 16);
  }
  lc.tok0 = 8 * (g & 1) + (g >> 1);
  lc.pmask = ((g & 1) == (c & 1)) ? 0xffffffffu : 0u;
  const int loff = (lane >> 5) * ROW_BYTES + (lane & 31) * 16;
  const char* kl = kc + loff;
  const char* vl = vc + loff;
  u32x4 q[8];
  float m, l;
  f32x4 acc[8];
  Tile ta, tb;
  if (__builtin_expect(wg < Ns, 0)) {
    const int kvh = wg & 7, sp = wg >> 3;
    const int p0 = min(sp * per, S), n = min(p0 + per, S) - p0;
    const int qr = c >> 1, row = (4 * wave + (qr >> 1)) * H + 2 * kvh + (qr & 1);
    if (wave < n) { const size_t pb_ = (size_t)lds_bt[p0 + wave] * PAGE_BYTES + kvh * 512; issue<1>(ta, kl + pb_, vl + pb_); }
#pragma unroll
    for (int j = 0; j < 8; ++j) q[j] = *reinterpret_cast<const u32x4*>(qg + (size_t)row * (D * 2) + (4 * j + g) * 16);
    m = -INFINITY; l = 0.f;
#pragma unroll
    for (int u = 0; u < 8; ++u) acc[u] = f32x4{0.f, 0.f, 0.f, 0.f};
    for (int r = 0; r < n; r += NW) {
      char* st = big + wave * 16384;
      if (r + wave < n) {
        kwrite(ta.k, st, lc);
#pragma unroll
        for (int j = 0; j < 8; ++j) *reinterpret_cast<u32x4*>(st + 8192 + j * 1024 + lane * 16) = vop(ta.v, j);
      }
      if (r + NW + wave < n) { const size_t pb_ = (size_t)lds_bt[p0 + r + NW + wave] * PAGE_BYTES + kvh * 512; issue<1>(ta, kl + pb_, vl + pb_); }
      __syncthreads();
      const int cnt = min(NW, n - r);
      for (int pg = 0; pg < cnt; ++pg) {
        const char* sp_ = big + pg * 16384;
#pragma unroll
        for (int j = 0; j < 8; ++j) tb.v[j] = *reinterpret_cast<const u32x4*>(sp_ + 8192 + j * 1024 + lane * 16);
        core<true>(tb.v, q, BS, sp_, lc, m, l, acc);
      }
      __syncthreads();
    }
    l = xrow16_sum(l);
    const size_t rs = (size_t)row * NSLOT + NPR + sp;
    {
      float* tr = reinterpret_cast<float*>(big + wave * 16384);
#pragma unroll
      for (int i = 0; i < 4; ++i) {
        float* dst = tr + qr * (D + 4) + 128 * (c & 1) + 32 * g + 8 * i;
        *reinterpret_cast<f32x4*>(dst) = f32x4{acc[0][i], acc[1][i], acc[2][i], acc[3][i]};
        *reinterpret_cast<f32x4*>(dst + 4) = f32x4{acc[4][i], acc[5][i], acc[6][i], acc[7][i]};
      }
      __builtin_amdgcn_wave_barrier();
#pragma unroll
      for (int r = 0; r < 8; ++r) {
        const size_t rr = (size_t)((4 * wave + (r >> 1)) * H + 2 * kvh + (r & 1)) * NSLOT + NPR + sp;
        stw(ws_acc + rr * D + lane * 4, *reinterpret_cast<const f32x4*>(tr + r * (D + 4) + lane * 4));
      }
      __builtin_amdgcn_wave_barrier();
    }
    if (g == 0 && (c & 1) == 0) *reinterpret_cast<f32x2*>(ws_ml + rs * 2) = f32x2{m, l};
    __syncthreads();
  }
  char* ks = big + wave * 8192;
  static_assert(NACT * (8192 + NPC * 1152) <= 65536, "K scratch + q copies");
  char* qw = big + NACT * 8192 + wave * (NPC * 1152);
  const char* qb = c < 4 ? qw + (c >> 1) * 576 + g * 16 : lds_z + g * 16;
  const int qst = c < 4 ? 1152 : 0;
  const char* qs = qb;
  int u = wg < Ns ? wg * Us : usplit + (wg - Ns) * U;
  const int u1 = min(u + (wg < Ns ? Us : U), Tp);
  bool first = true;
  while (u < u1) {
    const int uu = u + wave + NACT * lane;
    int offv = 0, remv = BS, pcv = NPC, ue = u, rowp = 0, slotp = 0, npcs = 0, qrow = 0;
#pragma unroll
    for (int p = 0; p < NPC; ++p) {
      if (ue < u1) {
        const int b = __builtin_ctzll(__builtin_amdgcn_ballot_w64(lane < 32 && 8 * incl > ue));
        const int pb = __builtin_amdgcn_readlane(priv, b), st = 8 * (__builtin_amdgcn_readlane(incl, b) - pb);
        const int lb = __builtin_amdgcn_readlane(len, b);
        const int off = ue - st, kvh = __builtin_popcountll(__builtin_amdgcn_ballot_w64(lane < 7 && (lane + 1) * pb <= off)), pg0 = off - kvh * pb, np = min(pb - pg0, u1 - ue);
        if (uu >= ue && uu < ue + np) {
          const int lp = S + pg0 + (uu - ue);
          offv = lds_bt[b * NB + lp] * PAGE_BYTES + kvh * 512; remv = lb - lp * BS; pcv = p;
        }
        if (lane == p) qrow = b * H + 2 * kvh;
        if ((tid >> 7) == p) { rowp = b * H + 2 * kvh; slotp = st + kvh * pb; }
        ue += np; npcs = p + 1;
      }
    }
    const int cnt = ue - u;
    const int metav = min(remv, BS) | (pcv << 8);
    if (lane < NPC * 2) { lds_ml[lane >> 1][wave][lane & 1][0] = -INFINITY; lds_ml[lane >> 1][wave][lane & 1][1] = 0.f; }
#define LOADQ(P) do { qs = qb + (P) * qst; m = -INFINITY; l = 0.f; _Pragma("unroll") for (int v = 0; v < 8; ++v) acc[v] = f32x4{0.f, 0.f, 0.f, 0.f}; } while (0)
#define FLUSH(P) do { l = xrow16_sum(l); if (c < 4) { \
      float* mrg = reinterpret_cast<float*>(big + 65536 + ((P) * NW + wave) * 2048) + (c >> 1) * D + 128 * (c & 1) + 32 * g; \
      _Pragma("unroll") for (int i = 0; i < 4; ++i) { \
        *reinterpret_cast<f32x4*>(mrg + 8 * i) = f32x4{acc[0][i], acc[1][i], acc[2][i], acc[3][i]}; \
        *reinterpret_cast<f32x4*>(mrg + 8 * i + 4) = f32x4{acc[4][i], acc[5][i], acc[6][i], acc[7][i]}; } \
      if (g == 0 && (c & 1) == 0) { lds_ml[P][wave][c >> 1][0] = m; lds_ml[P][wave][c >> 1][1] = l; } } } while (0)
    if (wave < min(cnt, NACT)) {
#pragma unroll
      for (int p = 0; p < NPC; ++p) q[p] = *reinterpret_cast<const u32x4*>(qg + (size_t)__builtin_amdgcn_readlane(qrow, p) * (D * 2) + lane * 16);
      const unsigned pb_ = (unsigned)__builtin_amdgcn_readlane(offv, 0);
      issue_c(ta, kc + pb_, vc + pb_, lane, (__builtin_amdgcn_readlane(metav, 0) & 255) - 1);
#pragma unroll
      for (int p = 0; p < NPC; ++p) *reinterpret_cast<u32x4*>(qw + p * 1152 + (lane >> 5) * 576 + (lane & 31) * 16) = q[p];
    }
    slotp = wg - wg_of(slotp);
    if (first) { first = false;
      {
        const int b = wg >> 3, kvh = wg & 7;
        const int pb = __builtin_amdgcn_readlane(priv, b), st = 8 * (__builtin_amdgcn_readlane(incl, b) - pb) + kvh * pb;
        const int npc = pb > 0 ? wg_of(st + pb - 1) - wg_of(st) + 1 : 0;
        const int slot = wave;
        if ((slot < NPR && slot >= npc) || (slot >= NPR && slot < NPR + NSH && Ns == 0)) {
    #pragma unroll
          for (int h = 0; h < 2; ++h) {
            const size_t rs = (size_t)(b * H + 2 * kvh + h) * NSLOT + slot;
            if (lane == 0) *reinterpret_cast<f32x2*>(ws_ml + rs * 2) = f32x2{-INFINITY, 0.f};
          }
        }
      }
     }
    if (wave < min(cnt, NACT)) {
      int meta = __builtin_amdgcn_readlane(metav, 0), pc = meta >> 8;
      LOADQ(pc);
      for (int i = 1;; ++i) {
        kwrite(ta.k, ks, lc);
        const unsigned noff = (unsigned)__builtin_amdgcn_readlane(offv, i);
        const int nmeta = __builtin_amdgcn_readlane(metav, i);
#pragma unroll
        for (int j = 0; j < 8; ++j) tb.v[j] = ta.v[j];
        if (nmeta < (NPC << 8)) issue_c(ta, kc + noff, vc + noff, lane, (nmeta & 255) - 1);
#pragma unroll
        for (int j = 0; j < 8; ++j) q[j] = *reinterpret_cast<const u32x4*>(qs + j * 64);
        core<false>(tb.v, q, meta & 255, ks, lc, m, l, acc);
        const int pn = nmeta >> 8;
        if (pn != pc) { FLUSH(pc); pc = pn; if (pc >= NPC) break; LOADQ(pc); }
        meta = nmeta;
      }
    }
#undef LOADQ
#undef FLUSH
    __syncthreads();
    {
      const int p = tid >> 7, h = (tid >> 6) & 1;
      if (p < npcs) {
        f32x2 mlw[NW]; f32x4 aw[NW];
#pragma unroll
        for (int w = 0; w < NW; ++w) {
          mlw[w] = *reinterpret_cast<const f32x2*>(lds_ml[p][w][h]);
          aw[w] = *reinterpret_cast<const f32x4*>(big + 65536 + (p * NW + w) * 2048 + h * 1024 + lane * 16);
        }
        float M = -INFINITY;
#pragma unroll
        for (int w = 0; w < NW; ++w) M = fmaxf(M, mlw[w][0]);
        float L = 0.f; f32x4 o = {0.f, 0.f, 0.f, 0.f};
#pragma unroll
        for (int w = 0; w < NW; ++w) {
          const bool ok = mlw[w][1] > 0.f;
          const float wt = ok ? __builtin_amdgcn_exp2f(mlw[w][0] - M) : 0.f;
          L = fmaf(wt, mlw[w][1], L);
#pragma unroll
          for (int e = 0; e < 4; ++e) o[e] = fmaf(ok ? aw[w][e] : 0.f, wt, o[e]);
        }
        const size_t rs = (size_t)(rowp + h) * NSLOT + slotp;
        stw(ws_acc + rs * D + lane * 4, o);
        if (lane == 0) *reinterpret_cast<f32x2*>(ws_ml + rs * 2) = f32x2{M, L};
      }
    }
    u = ue;
    if (u < u1) __syncthreads();
  }
  asm volatile("" :: "v"(pf));
  if (first) {
  {
    const int b = wg >> 3, kvh = wg & 7;
    const int pb = __builtin_amdgcn_readlane(priv, b), st = 8 * (__builtin_amdgcn_readlane(incl, b) - pb) + kvh * pb;
    const int npc = pb > 0 ? wg_of(st + pb - 1) - wg_of(st) + 1 : 0;
    const int slot = wave;
    if ((slot < NPR && slot >= npc) || (slot >= NPR && slot < NPR + NSH && Ns == 0)) {
#pragma unroll
      for (int h = 0; h < 2; ++h) {
        const size_t rs = (size_t)(b * H + 2 * kvh + h) * NSLOT + slot;
        if (lane == 0) *reinterpret_cast<f32x2*>(ws_ml + rs * 2) = f32x2{-INFINITY, 0.f};
      }
    }
  }
  }
}

__global__ void __launch_bounds__(256)
attn_combine(const char* __restrict__ ws, const char* __restrict__ vc, const int* __restrict__ bt, const int* __restrict__ lens,
             __hip_bfloat16* __restrict__ out) {
  const int lane = threadIdx.x & 63, row = blockIdx.x * 4 + (threadIdx.x >> 6);
  const float* ml = reinterpret_cast<const float*>(ws + WS_ML) + (size_t)row * NSLOT * 2;
  const float* ac = reinterpret_cast<const float*>(ws + WS_ACC) + (size_t)row * NSLOT * D + lane * 4;
  constexpr int NS = NPR + NSH;
  f32x2 mlv[NS]; f32x4 a[NS];
#pragma unroll
  for (int s = 0; s < NS; ++s) { mlv[s] = *reinterpret_cast<const f32x2*>(ml + 2 * s); a[s] = ldw<f32x4>(ac + s * D); }
  const int b = row / H;
  const int len = min(max(lens[b], 0), LK);
  float M = -INFINITY;
#pragma unroll
  for (int s = 0; s < NS; ++s) M = fmaxf(M, mlv[s][0]);
  float L = 0.f; f32x4 o = {0.f, 0.f, 0.f, 0.f};
#pragma unroll
  for (int s = 0; s < NS; ++s) {
    const bool ok = mlv[s][1] > 0.f;
    const float wt = ok ? __builtin_amdgcn_exp2f(mlv[s][0] - M) : 0.f;
    L = fmaf(wt, mlv[s][1], L);
#pragma unroll
    for (int e = 0; e < 4; ++e) o[e] = fmaf(ok ? a[s][e] : 0.f, wt, o[e]);
  }
  const float MK = MASKED * LOG2E;
  if (len < LK && !(M > MK + 200.f)) {
    const float M2 = fmaxf(M, MK);
    const float w1 = (L > 0.f) ? __builtin_amdgcn_exp2f(M - M2) : 0.f, w2 = __builtin_amdgcn_exp2f(MK - M2);
    f32x4 sv = {0.f, 0.f, 0.f, 0.f};
    const int kvh = (row % H) >> 1;
    for (int t = len; t < LK; ++t) {
      const ushort4 r = *reinterpret_cast<const ushort4*>(vc + (size_t)bt[b * NB + (t >> 4)] * PAGE_BYTES + (size_t)(t & 15) * ROW_BYTES + kvh * 512 + lane * 8);
      sv[0] += __uint_as_float((unsigned)r.x << 16); sv[1] += __uint_as_float((unsigned)r.y << 16);
      sv[2] += __uint_as_float((unsigned)r.z << 16); sv[3] += __uint_as_float((unsigned)r.w << 16);
    }
    o = o * w1 + sv * w2; L = L * w1 + (float)(LK - len) * w2;
  }
  const float inv = 1.f / L;
  uint2 r = {cvtpk(o[0] * inv, o[1] * inv), cvtpk(o[2] * inv, o[3] * inv)};
  *reinterpret_cast<uint2*>(out + (size_t)row * D + lane * 4) = r;
}

extern "C" void kernel_launch(void* const* d_in, const int* in_sizes, int n_in, void* d_out, int out_size,
                              void* d_ws, size_t ws_size, hipStream_t stream) {
  attn_split<<<NWG, NW * 64, 0, stream>>>((const char*)d_in[0], (const char*)d_in[1], (const char*)d_in[2],
                                          (const int*)d_in[3], (const int*)d_in[4], (char*)d_ws);
  attn_combine<<<B * H / 4, 256, 0, stream>>>((const char*)d_ws, (const char*)d_in[2], (const int*)d_in[3], (const int*)d_in[4],
                                              (__hip_bfloat16*)d_out);
}
```
